```python
import torch

def _softmax(x, axis=-1):
    return torch.softmax(x, dim=axis)
def _gelu(x):
    return torch.nn.functional.gelu(x, approximate="tanh")
def _silu(x):
    return torch.nn.functional.silu(x)
def _sigmoid(x):
    return torch.sigmoid(x)
def _layernorm(x, axis=-1, eps=1e-5):
    m = x.mean(dim=axis, keepdim=True)
    v = x.var(dim=axis, keepdim=True, unbiased=False)
    return (x - m) / torch.sqrt(v + eps)
def _scatter_add(tgt, idx, src, axis=0):
    return tgt.clone().index_add_(axis, idx, src)
def _segment_sum(src, seg, n):
    out = torch.zeros((n,) + src.shape[1:], dtype=src.dtype)
    return out.index_add_(0, seg, src)
def _pad(x, pw):
    flat = tuple(v for pair in reversed(pw) for v in pair)
    return torch.nn.functional.pad(x, flat)

kernel_name = 'concat_heads__linear_attn_block__spectral_gfnet__prompt'

def setup_inputs():

    _w = (lambda a: (lambda t: t.to(torch.bfloat16) if t.is_floating_point() else t)(torch.as_tensor(a)))
    def _A_setup():
        B, S, D = 1, 2048, 512
        return {
            "x": _w((lambda *s: torch.randn(*s, dtype=torch.bfloat16) * 0.05)(B, S, D)),
            "wqkv": _w((lambda *s: torch.randn(*s, dtype=torch.bfloat16) * 0.4)(D, 3 * D)),
            "wo": _w((lambda *s: torch.randn(*s, dtype=torch.bfloat16) * 0.05)(D, D)),
        }
    _A_in = _A_setup()

    def _B_setup():
        B, S, D = 1, 2048, 512
        Df = D // 2 + 1
        return {
            "x": _w((lambda *s: torch.randn(*s, dtype=torch.bfloat16) * 0.05)(B, S, D)),
            "w_r": _w((lambda *s: torch.randn(*s, dtype=torch.bfloat16) * 1.6)(S, Df) * 0.02),
            "w_i": _w((lambda *s: torch.randn(*s, dtype=torch.bfloat16) * 1.6)(S, Df) * 0.02),
        }
    _B_in = _B_setup()

    return {
        "A_x": _A_in["x"],
        "A_wqkv": _A_in["wqkv"],
        "A_wo": _A_in["wo"],
        "B_x": _B_in["x"],
        "B_w_r": _B_in["w_r"],
        "B_w_i": _B_in["w_i"],
    }

def reference(A_x, A_wqkv, A_wo, B_x, B_w_r, B_w_i):
    def _A_ref(x, wqkv, wo):
        B, S, D = x.shape
        H = 32; Dh = D // H
        qkv = (lambda x, *d: x.permute(*d))((x @ wqkv).reshape(B, S, 3, H, Dh), 2, 0, 3, 1, 4)
        q, k, v = qkv[0], qkv[1], qkv[2]
        phi = lambda t: torch.where(t > 0, t + 1, torch.exp(t)) + 1e-6
        pq, pk = phi(q), phi(k)
        kv = torch.einsum("bhsd,bhse->bhde", pk, v)
        z = torch.einsum("bhsd->bhd", pk)
        num = torch.einsum("bhqd,bhde->bhqe", pq, kv)
        den = torch.einsum("bhqd,bhd->bhq", pq, z)[..., None] + 1e-6
        o = num / den
        return (lambda x, *d: x.permute(*d))(o, 0, 2, 1, 3).reshape(B, S, D) @ wo

    def _B_ref(x, w_r, w_i):
        B, S, D = x.shape
        X = (lambda x, axes=(-2,-1): torch.fft.rfft2(x.float(), dim=axes))(x, axes=(-2, -1))
        W = w_r + 1j * w_i
        X = X * W
        y = (lambda x, s, axes=(-2,-1): torch.fft.irfft2(x, s=s, dim=axes).to(torch.bfloat16))(X, (S, D), axes=(-2, -1))
        return y

    a = _A_ref(A_x, A_wqkv, A_wo)
    b = _B_ref(B_x, B_w_r, B_w_i)
    return torch.concatenate([a, b], axis=-1)
```

```cpp
#include <hip/hip_runtime.h>
#include <stdint.h>

#define DI __device__ __forceinline__
#define LAS __attribute__((address_space(3)))
#define GAS __attribute__((address_space(1)))
typedef __bf16 bf16x8 __attribute__((ext_vector_type(8)));
typedef __bf16 bf16x2 __attribute__((ext_vector_type(2)));
typedef short s16x4 __attribute__((ext_vector_type(4)));
typedef float f32x2 __attribute__((ext_vector_type(2)));
typedef float f32x4 __attribute__((ext_vector_type(4)));
typedef float f32x16 __attribute__((ext_vector_type(16)));
typedef unsigned u32x2 __attribute__((ext_vector_type(2)));
typedef unsigned u32x4 __attribute__((ext_vector_type(4)));
typedef unsigned short u16;

constexpr size_t MB = 1u << 20;
constexpr size_t T_OFF = 0;
constexpr size_t U_OFF = 2 * MB;
constexpr size_t O_OFF = 4 * MB;
constexpr size_t PQ_OFF = 6 * MB;
constexpr size_t KV_OFF = 8 * MB;

DI unsigned pk(float lo, float hi) { f32x2 v = {lo, hi}; return __builtin_bit_cast(unsigned, __builtin_convertvector(v, bf16x2)); }
DI float bfr(float x) { return __builtin_bit_cast(float, pk(0.f, x) & 0xffff0000u); }
DI f32x4 mfma16(u32x4 a, u32x4 b, f32x4 c) { return __builtin_amdgcn_mfma_f32_16x16x32_bf16(__builtin_bit_cast(bf16x8, a), __builtin_bit_cast(bf16x8, b), c, 0, 0, 0); }
DI f32x16 mfma32(u32x4 a, u32x4 b, f32x16 c) { return __builtin_amdgcn_mfma_f32_32x32x16_bf16(__builtin_bit_cast(bf16x8, a), __builtin_bit_cast(bf16x8, b), c, 0, 0, 0); }
DI float cosr(float rev) { return __builtin_amdgcn_cosf(rev); }
DI float sinr(float rev) { return __builtin_amdgcn_sinf(rev); }
DI u32x2 lds_tr(const LAS char* p) { return __builtin_bit_cast(u32x2, __builtin_amdgcn_ds_read_tr16_b64_v4i16((LAS s16x4*)p)); }
DI u32x4 cat(u32x2 a, u32x2 b) { return u32x4{a.x, a.y, b.x, b.y}; }
#define WAITV(n) asm volatile("s_waitcnt vmcnt(" #n ")" ::: "memory")
#define WAITL() asm volatile("s_waitcnt lgkmcnt(0)" ::: "memory")
DI void bar() { asm volatile("s_waitcnt lgkmcnt(0)\n\ts_barrier" ::: "memory"); }
DI void glds(const void* sbase, unsigned voff, unsigned lds) {
    unsigned keep;
    asm volatile("s_mov_b32 %0, m0\n\ts_mov_b32 m0, %3\n\ts_nop 0\n\tglobal_load_lds_dwordx4 %1, %2\n\ts_mov_b32 m0, %0"
                 : "=&s"(keep) : "v"(voff), "s"(sbase), "s"(lds) : "memory");
}
DI unsigned ldsaddr(const LAS char* p) { return (unsigned)(uintptr_t)p; }
DI float phi(float t) { float a = bfr(t); float r = a > 0.f ? a + 1.f : __expf(a); return bfr(r) + 1e-6f; }

struct Params {
    const char* ax; const char* wqkv; const char* wo; const char* bx; const char* wr; const char* wi;
    char* out; char* ws;
};

DI void gcoef(int c, int nb, int kb, float& rr, float& ir, float& ri, float& ii) {
    const int kk = (c == 0 && kb >= 8) ? 16 + 32 * kb : c + 32 * kb;
    const float a = (float)((nb * kk) & 511) * (1.f / 512.f);
    const float co = cosr(a), si = sinr(a);
    const float sg = kb < 8 ? 1.f : -1.f;
    rr = co; ir = si; ri = -sg * si; ii = sg * co;
    if (c == 0) {
        if (kb < 8) { ir = 0.f; ii = 0.f; if (kb == 0) ri = (nb & 1) ? -1.f : 1.f; }
        else { rr = 0.f; ri = 0.f; ir = co; ii = si; }
    }
}
DI float ecoef(int na, int p) {
    const int c = p >> 1, im = p & 1;
    const int m = (im && c == 0) ? na * 16 : na * c + 8 * im;
    return cosr((float)(m & 31) * (1.f / 32.f));
}

constexpr int B1_XIN = 0, B1_EX = 16 * 1152, B1_LDS = B1_EX + 16384;
DI void b1_issue(const Params& P, LAS char* sm, int s_lo, int w, int l) {
    const char* base = P.bx + (size_t)(s_lo + 256 * w) * 1024;
    glds(base, l * 16, ldsaddr(sm) + B1_XIN + (2 * w) * 1152);
    glds(base + 128 * 1024, l * 16, ldsaddr(sm) + B1_XIN + (2 * w + 1) * 1152);
}
struct B1Mat { u32x4 e[2], gr[2], gi[2], hr, hi; };
DI void b1_mats(B1Mat& M, int s_lo, int w, int l) {
    const int h = l >> 5, g = l >> 4, i16 = l & 15;
#pragma unroll
    for (int t = 0; t < 2; ++t)
#pragma unroll
        for (int j = 0; j < 4; ++j) M.e[t][j] = pk(ecoef(16 * t + 8 * h + 2 * j, l & 31), ecoef(16 * t + 8 * h + 2 * j + 1, l & 31));
#pragma unroll
    for (int cc = 0; cc < 2; ++cc)
#pragma unroll
        for (int j = 0; j < 4; ++j) {
            float rr0, ir0, ri0, ii0, rr1, ir1, ri1, ii1;
            gcoef(2 * w + cc, 8 * (g & 1) + 2 * j, i16, rr0, ir0, ri0, ii0);
            gcoef(2 * w + cc, 8 * (g & 1) + 2 * j + 1, i16, rr1, ir1, ri1, ii1);
            M.gr[cc][j] = g < 2 ? pk(rr0, rr1) : pk(ir0, ir1);
            M.gi[cc][j] = g < 2 ? pk(ri0, ri1) : pk(ii0, ii1);
        }
#pragma unroll
    for (int j = 0; j < 4; ++j) {
        const float a = (float)(((s_lo + 128 * (4 * g + j)) * i16) & 2047) * (1.f / 2048.f);
        const float co = cosr(a), si = sinr(a);
        M.hr[j] = pk(co, si); M.hi[j] = pk(-si, co);
    }
}
DI void b1_run(const Params& P, LAS char* sm, const B1Mat& M, int s_lo, int w, int l) {
    const int h = l >> 5, g = l >> 4, i16 = l & 15;
    {
        const int q = i16 >> 2, p = i16 & 3, ps = ((p & 1) << 1) | (p >> 1);
        const LAS char* xr = sm + B1_XIN + (2 * w + (g & 1)) * 1152 + (8 * h + q) * 32 + ps * 8;
        const u32x4 a0 = cat(lds_tr(xr), lds_tr(xr + 128));
        const u32x4 a1 = cat(lds_tr(xr + 512), lds_tr(xr + 640));
        f32x16 acc = {};
        acc = mfma32(a0, M.e[0], acc);
        acc = mfma32(a1, M.e[1], acc);
        const int c = (l & 31) >> 1, qq = 2 * (l & 1) + h, ph = (c & 1) | ((c & 2) << 1);
#pragma unroll
        for (int sp = 0; sp < 2; ++sp) {
            const int s = 2 * w + sp;
            const int slot = ((s & 3) * 4 + (qq ^ ((s >> 3) << 1))) ^ ph;
            u32x4 v = {pk(acc[8 * sp], acc[8 * sp + 1]), pk(acc[8 * sp + 2], acc[8 * sp + 3]), pk(acc[8 * sp + 4], acc[8 * sp + 5]), pk(acc[8 * sp + 6], acc[8 * sp + 7])};
            *(LAS u32x4*)(sm + B1_EX + c * 1024 + (s >> 2) * 256 + slot * 16) = v;
        }
    }
    bar();
    unsigned tv[2][4];
#pragma unroll
    for (int cc = 0; cc < 2; ++cc) {
        const int c = 2 * w + cc, ph = (c & 1) | ((c & 2) << 1), s = i16;
        const int slot = ((s & 3) * 4 + (g ^ ((s >> 3) << 1))) ^ ph;
        const u32x4 a = *(const LAS u32x4*)(sm + B1_EX + c * 1024 + (s >> 2) * 256 + slot * 16);
        const f32x4 z = {};
        const f32x4 re = mfma16(a, M.gr[cc], z), im = mfma16(a, M.gi[cc], z);
        const u32x4 b = {pk(re[0], im[0]), pk(re[1], im[1]), pk(re[2], im[2]), pk(re[3], im[3])};
        const f32x4 tr = mfma16(M.hr, b, z), ti = mfma16(M.hi, b, z);
#pragma unroll
        for (int r = 0; r < 4; ++r) tv[cc][r] = pk(tr[r], ti[r]);
    }
    {
        const int q = w >> 1;
#pragma unroll
        for (int r = 0; r < 4; ++r)
            *(LAS u32x2*)(sm + B1_XIN + ((4 * g + r) * 4 + q) * 256 + ((i16 ^ (4 * q)) * 16) + (w & 1) * 8) = u32x2{tv[0][r], tv[1][r]};
    }
    bar();
#pragma unroll
    for (int it = 0; it < 2; ++it) {
        const int i = w * 64 + l + 512 * it, ch = i >> 2, q = i & 3;
        const u32x4 v = *(const LAS u32x4*)(sm + B1_XIN + ((ch >> 4) * 4 + q) * 256 + (((ch & 15) ^ (4 * q)) * 16));
        *(GAS u32x4*)(P.ws + T_OFF + (size_t)ch * 8192 + s_lo * 64 + q * 16) = v;
    }
}

constexpr int A1_RING = B1_LDS, A1_XS = 16384, A1_STAGE = 16384 + 12288, K1_LDS = A1_RING + 4 * A1_STAGE;
struct A1Ld { unsigned xo[4], wo[3]; };
DI void a1_ldinit(A1Ld& L, int rb, int hp, int lw, int l) {
#pragma unroll
    for (int m = 0; m < 4; ++m) {
        const int row = 32 * lw + 8 * m + (l >> 3);
        L.xo[m] = (rb * 128 + row) * 1024 + (((l & 7) ^ ((row >> 1) & 7)) * 16);
    }
#pragma unroll
    for (int n = 0; n < 3; ++n) {
        const int p = 64 * (3 * lw + n) + l, row = p / 12, seg = p - 12 * row;
        L.wo[n] = row * 3072 + (seg >> 2) * 1024 + hp * 64 + (seg & 3) * 16;
    }
}
DI void a1_stage(const Params& P, LAS char* sm, const A1Ld& L, int st, int lw) {
    const unsigned lb = ldsaddr(sm) + A1_RING + (st & 3) * A1_STAGE;
    const char* xb = P.ax + st * 128;
    const char* wb = P.wqkv + (size_t)st * 64 * 3072;
#pragma unroll
    for (int m = 0; m < 4; ++m) glds(xb, L.xo[m], lb + (4 * lw + m) * 1024);
#pragma unroll
    for (int n = 0; n < 3; ++n) glds(wb, L.wo[n], lb + A1_XS + (3 * lw + n) * 1024);
}

__global__ void __launch_bounds__(512, 2) k1(Params P) {
    __shared__ __attribute__((aligned(16))) char smem_[K1_LDS];
    LAS char* sm = (LAS char*)smem_;
    const int l = threadIdx.x & 63, w = __builtin_amdgcn_readfirstlane(threadIdx.x >> 6);
    const int b = blockIdx.x, xc = b & 7, j = b >> 3;
    const int rb = 4 * (xc >> 1) + (j >> 3), hp = 8 * (xc & 1) + (j & 7);
    const bool isB = j < 16;
    const int s_lo = xc * 16 + (j & 15);
    if (isB) b1_issue(P, sm, s_lo, w, l);
    A1Ld L;
    if (w >= 4) {
        a1_ldinit(L, rb, hp, w - 4, l);
#pragma unroll
        for (int st = 0; st < 4; ++st) a1_stage(P, sm, L, st, w - 4);
    }
    if (isB) {
        B1Mat M;
        b1_mats(M, s_lo, w, l);
        if (w >= 4) WAITV(28); else WAITV(0);
        b1_run(P, sm, M, s_lo, w, l);
    }
    if (w >= 4) {
        WAITV(21); bar();
        WAITV(14); bar(); a1_stage(P, sm, L, 4, w - 4);
        WAITV(14); bar(); a1_stage(P, sm, L, 5, w - 4);
        WAITV(14); bar(); a1_stage(P, sm, L, 6, w - 4);
        WAITV(14); bar(); a1_stage(P, sm, L, 7, w - 4);
        WAITV(14); bar();
        WAITV(7); bar();
        WAITV(0); bar();
        bar();
    } else {
        const int h = l >> 5, G = l >> 4, q = (l & 15) >> 2, p = l & 3;
        const int row = 32 * w + (l & 31);
        const int xoff = row * 128, xsw = (row >> 1) & 7;
        const int woff = A1_XS + (8 * h + q) * 192 + (G & 1) * 32 + p * 8;
        f32x16 aq = {}, ak = {}, av = {};
        for (int st = 0; st < 8; ++st) {
            bar();
            const LAS char* sb = sm + A1_RING + (st & 3) * A1_STAGE;
#pragma unroll
            for (int t = 0; t < 4; ++t) {
                const u32x4 xa = *(const LAS u32x4*)(sb + xoff + (((2 * t + h) ^ xsw) * 16));
                const LAS char* wp = sb + woff + t * 16 * 192;
                const u32x4 wq = cat(lds_tr(wp), lds_tr(wp + 768));
                const u32x4 wk = cat(lds_tr(wp + 64), lds_tr(wp + 64 + 768));
                const u32x4 wv = cat(lds_tr(wp + 128), lds_tr(wp + 128 + 768));
                ak = mfma32(xa, wk, ak);
                av = mfma32(xa, wv, av);
                aq = mfma32(wq, xa, aq);
            }
        }
        f32x16 kv = {}, zz = {};
        const u32x4 ones = {0x3f803f80u, 0x3f803f80u, 0x3f803f80u, 0x3f803f80u};
#pragma unroll
        for (int s = 0; s < 2; ++s) {
            u32x4 pkf, vf;
#pragma unroll
            for (int jj = 0; jj < 4; ++jj) {
                pkf[jj] = pk(phi(ak[8 * s + 2 * jj]), phi(ak[8 * s + 2 * jj + 1]));
                vf[jj] = pk(av[8 * s + 2 * jj], av[8 * s + 2 * jj + 1]);
            }
            kv = mfma32(pkf, vf, kv);
            zz = mfma32(pkf, ones, zz);
        }
        const bool hi = (l & 16) != 0;
        f32x4 k0, k1v, z0, z1;
#pragma unroll
        for (int r = 0; r < 4; ++r) {
            k0[r] = hi ? kv[8 + r] : kv[r]; k1v[r] = hi ? kv[12 + r] : kv[4 + r];
            z0[r] = hi ? zz[8 + r] : zz[r]; z1[r] = hi ? zz[12 + r] : zz[4 + r];
        }
        LAS f32x4* red = (LAS f32x4*)(sm + B1_EX) + w * 136;
        red[2 * l] = k0; red[2 * l + 1] = k1v;
        if ((l & 15) == 0) { const int zi = 128 + ((hi ? 2 : 0) + h) * 2; red[zi] = z0; red[zi + 1] = z1; }
#pragma unroll
        for (int s = 0; s < 2; ++s) {
            u32x4 f;
#pragma unroll
            for (int jj = 0; jj < 4; ++jj) f[jj] = pk(phi(aq[8 * s + 2 * jj]), phi(aq[8 * s + 2 * jj + 1]));
            *(GAS u32x4*)(P.ws + PQ_OFF + (size_t)((b * 4 + w) * 2 + s) * 1024 + l * 16) = f;
        }
        bar();
    }
    const int t = threadIdx.x;
    if (t < 136) {
        const LAS f32x4* red = (const LAS f32x4*)(sm + B1_EX) + t;
        const f32x4 s = (red[0] + red[136]) + (red[272] + red[408]);
        *(GAS f32x4*)(P.ws + KV_OFF + (size_t)((hp * 16 + rb) * 136 + t) * 16) = s;
    }
}

constexpr int B2_TIN = 0, B2_W = 8192, A2_KVF = 16384, K2_LDS = 16384 + 2048;
struct B2Mat { u32x4 f8r, f8i, f16r, f16i; float t1c[4], t1s[4], t2c[2], t2s[2]; };
DI void b2_mats(B2Mat& M, int l) {
    const int g = l >> 4, i16 = l & 15;
#pragma unroll
    for (int j = 0; j < 4; ++j) {
        const float a = (float)(((2 * g + (j >> 1)) * (i16 >> 1)) & 7) * 0.125f;
        const float co = cosr(a), si = sinr(a);
        const bool on = (j & 1) == (i16 & 1);
        M.f8r[j] = on ? pk(co, si) : 0u; M.f8i[j] = on ? pk(-si, co) : 0u;
        const float a2 = (float)(((4 * g + j) * i16) & 15) * 0.0625f;
        const float c2 = cosr(a2), s2 = sinr(a2);
        M.f16r[j] = pk(c2, s2); M.f16i[j] = pk(-s2, c2);
        const float a3 = (float)((4 * g + j) * (i16 >> 1)) * (1.f / 128.f);
        M.t1c[j] = cosr(a3); M.t1s[j] = sinr(a3);
    }
#pragma unroll
    for (int j = 0; j < 2; ++j) {
        const float a = (float)((2 * g + j) * i16) * (1.f / 128.f);
        M.t2c[j] = cosr(a); M.t2s[j] = sinr(a);
    }
}
DI void b2_tile(const B2Mat& M, u32x4 a, const float (&wr)[4], const float (&wi)[4], f32x4& ore, f32x4& oim) {
    const f32x4 z = {};
    f32x4 re = mfma16(a, M.f8r, z), im = mfma16(a, M.f8i, z);
    u32x4 b;
#pragma unroll
    for (int r = 0; r < 4; ++r) b[r] = pk(re[r] * M.t1c[r] + im[r] * M.t1s[r], im[r] * M.t1c[r] - re[r] * M.t1s[r]);
    re = mfma16(M.f16r, b, z); im = mfma16(M.f16i, b, z);
#pragma unroll
    for (int r = 0; r < 4; ++r) b[r] = pk(re[r] * wi[r] + im[r] * wr[r], re[r] * wr[r] - im[r] * wi[r]);
    im = mfma16(b, M.f16r, z); re = mfma16(b, M.f16i, z);
#pragma unroll
    for (int r = 0; r < 4; ++r) b[r] = pk(im[r] * M.t2c[r >> 1] + re[r] * M.t2s[r >> 1], re[r] * M.t2c[r >> 1] - im[r] * M.t2s[r >> 1]);
    oim = mfma16(M.f8r, b, z); ore = mfma16(M.f8i, b, z);
}
DI float bf2f(unsigned v) { return __builtin_bit_cast(float, v << 16); }

__global__ void __launch_bounds__(512, 2) k2(Params P) {
    __shared__ __attribute__((aligned(16))) char smem_[K2_LDS];
    LAS char* sm = (LAS char*)smem_;
    const int t = threadIdx.x, l = t & 63, w = __builtin_amdgcn_readfirstlane(t >> 6);
    const int b = blockIdx.x, xc = b & 7, j = b >> 3;
    const int g = l >> 4, i16 = l & 15, h = l >> 5;
    const int k_lo = xc + 8 * (j & 1), kb = j >> 1;
    const int jg = kb < 8 ? 2 * kb : 2 * (15 - kb) + 1;
    {
        const int s = 16 * w + (l >> 2);
        glds(P.ws + T_OFF + (size_t)(k_lo * 16 + kb) * 8192, s * 64 + (((l & 3) ^ ((s >> 2) & 3)) * 16), ldsaddr(sm) + B2_TIN + w * 1024);
    }
    typedef u32x4 u32x4u __attribute__((aligned(2)));
    const u32x4 wraw = *(const GAS u32x4u*)((t < 256 ? P.wr : P.wi) + (size_t)((k_lo + 16 * ((t >> 1) & 127)) * 257 + 16 * jg + 8 * (t & 1)) * 2);
    const int rb = 4 * (xc >> 1) + (j >> 3), hp = 8 * (xc & 1) + (j & 7);
    const GAS f32x4* kvp = (const GAS f32x4*)(P.ws + KV_OFF) + (size_t)hp * 16 * 136;
    f32x4 kv4[4], z4[2];
#pragma unroll
    for (int i = 0; i < 4; ++i) kv4[i] = kvp[(4 * i + g) * 136 + 16 * w + i16];
    if (w == 7) {
#pragma unroll
        for (int i = 0; i < 2; ++i) z4[i] = kvp[(8 * i + (l >> 3)) * 136 + 128 + (l & 7)];
    }
    u32x4 pq[2];
    if (w < 4) {
#pragma unroll
        for (int s = 0; s < 2; ++s) pq[s] = *(const GAS u32x4*)(P.ws + PQ_OFF + (size_t)((b * 4 + w) * 2 + s) * 1024 + l * 16);
    }
    const bool special = (kb == 0) && (w == 0);
    u32x4 spa = {}; float swr[4] = {}, swi[4] = {};
    if (special) {
        const GAS unsigned* t0 = (const GAS unsigned*)(P.ws + T_OFF + (size_t)(k_lo * 16) * 8192);
        const GAS unsigned* t1 = (const GAS unsigned*)(P.ws + T_OFF + (size_t)(((16 - k_lo) & 15) * 16) * 8192);
#pragma unroll
        for (int i = 0; i < 2; ++i) {
            const int s = 16 * (2 * g + i) + i16;
            spa[2 * i] = t0[s * 16]; spa[2 * i + 1] = t1[s * 16] ^ 0x80000000u;
        }
        const GAS u16* wr = (const GAS u16*)P.wr; const GAS u16* wi = (const GAS u16*)P.wi;
        const int u = i16 & 1;
        unsigned raw[4][8];
#pragma unroll
        for (int r = 0; r < 4; ++r) {
            int k1 = k_lo + 16 * ((i16 >> 1) + 8 * (4 * g + r));
            if (u && k_lo) k1 -= 16;
            const int ka = (k1 & 2047) * 257, kn = ((-k1) & 2047) * 257;
            raw[r][0] = wr[ka]; raw[r][1] = wr[kn]; raw[r][2] = wi[ka]; raw[r][3] = wi[kn];
            raw[r][4] = wr[ka + 256]; raw[r][5] = wr[kn + 256]; raw[r][6] = wi[ka + 256]; raw[r][7] = wi[kn + 256];
        }
        asm volatile("" ::: "memory");
#pragma unroll
        for (int r = 0; r < 4; ++r) {
            const float ar = bf2f(raw[r][0]) + bf2f(raw[r][1]), ai = bf2f(raw[r][2]) - bf2f(raw[r][3]);
            const float br = bf2f(raw[r][4]) + bf2f(raw[r][5]), bi = bf2f(raw[r][6]) - bf2f(raw[r][7]);
            const float sc = 0.25f / (2048.f * 512.f);
            swr[r] = (u ? ar - br : ar + br) * sc; swi[r] = (u ? ai - bi : ai + bi) * sc;
        }
    }
    B2Mat M;
    b2_mats(M, l);
    *(LAS u32x4*)(sm + B2_W + t * 16) = wraw;
    {
        f32x4 s = (kv4[0] + kv4[1]) + (kv4[2] + kv4[3]);
#pragma unroll
        for (int r = 0; r < 4; ++r) { s[r] += __shfl_xor(s[r], 16); s[r] += __shfl_xor(s[r], 32); }
        if (l < 16) *(LAS u32x2*)(sm + A2_KVF + (16 * w + l) * 8) = u32x2{pk(s[0], s[1]), pk(s[2], s[3])};
        if (w == 7) {
            f32x4 zs = z4[0] + z4[1];
#pragma unroll
            for (int r = 0; r < 4; ++r) { zs[r] += __shfl_xor(zs[r], 8); zs[r] += __shfl_xor(zs[r], 16); zs[r] += __shfl_xor(zs[r], 32); }
            if (l < 8) *(LAS u32x2*)(sm + A2_KVF + 1024 + l * 8) = u32x2{pk(zs[0], zs[1]), pk(zs[2], zs[3])};
        }
    }
    WAITV(0);
    bar();
    {
        const int c = 2 * w + (i16 & 1), col = kb < 8 ? c : ((16 - c) & 15);
        float wr[4], wi[4];
        const float sc = 2.f / (2048.f * 512.f);
#pragma unroll
        for (int r = 0; r < 4; ++r) {
            const int khi = (i16 >> 1) + 8 * (4 * g + r);
            wr[r] = bf2f(*(const LAS u16*)(sm + B2_W + khi * 32 + col * 2)) * sc;
            wi[r] = bf2f(*(const LAS u16*)(sm + B2_W + 4096 + khi * 32 + col * 2)) * sc;
        }
        LAS char* tp = sm + B2_TIN + (32 * g + i16) * 64 + (((w >> 1) ^ (i16 >> 2)) * 16) + (w & 1) * 8;
        const u32x4 a = cat(*(const LAS u32x2*)tp, *(const LAS u32x2*)(tp + 1024));
        f32x4 ore, oim;
        b2_tile(M, a, wr, wi, ore, oim);
        u32x4 o = {pk(ore[0], oim[0]), pk(ore[1], oim[1]), pk(ore[2], oim[2]), pk(ore[3], oim[3])};
        if (special) {
            b2_tile(M, spa, swr, swi, ore, oim);
#pragma unroll
            for (int i = 0; i < 2; ++i) {
                const float an = k_lo ? (float)(16 * (2 * g + i) + i16) * (1.f / 128.f) : 0.f;
                const float co = cosr(an), si = sinr(an);
                o[2 * i] = pk(ore[2 * i] + ore[2 * i + 1] * co + oim[2 * i + 1] * si, oim[2 * i] + oim[2 * i + 1] * co - ore[2 * i + 1] * si);
            }
        }
        *(LAS u32x2*)tp = u32x2{o[0], o[1]};
        *(LAS u32x2*)(tp + 1024) = u32x2{o[2], o[3]};
    }
    bar();
    {
        const int s = t >> 2;
        const u32x4 v = *(const LAS u32x4*)(sm + B2_TIN + t * 16);
        *(GAS u32x4*)(P.ws + U_OFF + (size_t)s * 16384 + (k_lo * 16 + kb) * 64 + (((t & 3) ^ ((s >> 2) & 3)) * 16)) = v;
    }
    if (w < 4) {
        const u32x4 f = *(const LAS u32x4*)(sm + A2_KVF + l * 16);
        const bool hi = (l & 16) != 0;
        const u32x4 zf = *(const LAS u32x4*)(sm + A2_KVF + 1024 + ((hi ? 2 : 0) + h) * 16);
        const u32x4 zero = {};
        f32x16 num = {}, den = {};
        num = mfma32(hi ? zero : f, pq[0], num); num = mfma32(hi ? f : zero, pq[1], num);
        den = mfma32(hi ? zero : zf, pq[0], den); den = mfma32(hi ? zf : zero, pq[1], den);
#pragma unroll
        for (int s = 0; s < 2; ++s) {
            const float rd = __builtin_amdgcn_rcpf(den[8 * s] + 1e-6f);
            u32x4 o;
#pragma unroll
            for (int jj = 0; jj < 4; ++jj) o[jj] = pk(num[8 * s + 2 * jj] * rd, num[8 * s + 2 * jj + 1] * rd);
            *(GAS u32x4*)(P.ws + O_OFF + (size_t)((rb * 4 + w) * 32 + 2 * hp + s) * 1024 + l * 16) = o;
        }
    }
}

constexpr int B3_UIN = 0, B3_EX = 16384, A3_WO = 32768, K3_LDS = 32768 + 65536;
__global__ void __launch_bounds__(512, 2) k3(Params P) {
    __shared__ __attribute__((aligned(16))) char smem_[K3_LDS];
    LAS char* sm = (LAS char*)smem_;
    const int t = threadIdx.x, l = t & 63, w = __builtin_amdgcn_readfirstlane(t >> 6);
    const int b = blockIdx.x, xc = b & 7, j = b >> 3;
    const int g = l >> 4, i16 = l & 15, h = l >> 5;
    const bool isB = j < 16;
    const int s_lo = xc * 16 + (j & 15);
    const int rt64 = 8 * (xc >> 1) + (j >> 2), ct = 4 * (xc & 1) + (j & 3);
    if (isB) {
#pragma unroll
        for (int i = 0; i < 2; ++i)
            glds(P.ws + U_OFF + (size_t)s_lo * 16384 + (2 * w + i) * 1024, (l >> 2) * 64 + (((l & 3) ^ (l >> 4)) * 16), ldsaddr(sm) + B3_UIN + (2 * w + i) * 1024);
    }
    {
        const unsigned vo = (l >> 3) * 1024 + ((l & 7) ^ ((l >> 2) & 4)) * 16;
#pragma unroll
        for (int m = 0; m < 8; ++m) glds(P.wo + (size_t)(8 * w + m) * 8 * 1024 + ct * 128, vo, ldsaddr(sm) + A3_WO + (8 * w + m) * 1024);
    }
    u32x4 of[32];
    const int rt32 = 2 * rt64 + (w & 1);
    if (w < 4) {
#pragma unroll
        for (int H = 0; H < 32; ++H) of[H] = *(const GAS u32x4*)(P.ws + O_OFF + (size_t)(rt32 * 32 + H) * 1024 + l * 16);
    }
    if (isB) {
        u32x4 hr, hi, gr[2], gi[2], e[2];
#pragma unroll
        for (int jj = 0; jj < 4; ++jj) {
            const float a = (float)(((s_lo + 128 * i16) * (4 * g + jj)) & 2047) * (1.f / 2048.f);
            const float co = cosr(a), si = sinr(a);
            hr[jj] = pk(co, -si); hi[jj] = pk(si, co);
#pragma unroll
            for (int cc = 0; cc < 2; ++cc) {
                float rr, ir, ri, ii;
                gcoef(2 * w + cc, i16, 4 * g + jj, rr, ir, ri, ii);
                gr[cc][jj] = pk(rr, ri); gi[cc][jj] = pk(ir, ii);
            }
#pragma unroll
            for (int tt = 0; tt < 2; ++tt) e[tt][jj] = pk(ecoef(l & 31, 16 * tt + 8 * h + 2 * jj), ecoef(l & 31, 16 * tt + 8 * h + 2 * jj + 1));
        }
        if (w < 4) WAITV(40); else WAITV(8);
        bar();
        u32x2 d[4];
#pragma unroll
        for (int i = 0; i < 4; ++i) d[i] = *(const LAS u32x2*)(sm + B3_UIN + (4 * g + i) * 1024 + i16 * 64 + (((w >> 1) ^ (i16 >> 2)) * 16) + (w & 1) * 8);
        unsigned vv[2][4];
#pragma unroll
        for (int cc = 0; cc < 2; ++cc) {
            const u32x4 a = {d[0][cc], d[1][cc], d[2][cc], d[3][cc]};
            const f32x4 z = {};
            const f32x4 re = mfma16(a, hr, z), im = mfma16(a, hi, z);
            const u32x4 a2 = {pk(re[0], im[0]), pk(re[1], im[1]), pk(re[2], im[2]), pk(re[3], im[3])};
            const f32x4 vr = mfma16(a2, gr[cc], z), vi = mfma16(a2, gi[cc], z);
#pragma unroll
            for (int r = 0; r < 4; ++r) vv[cc][r] = pk(vr[r], vi[r]);
        }
#pragma unroll
        for (int r = 0; r < 4; ++r)
            *(LAS u32x2*)(sm + B3_EX + (4 * g + r) * 1024 + i16 * 64 + (((w >> 1) ^ (i16 >> 2)) * 16) + (w & 1) * 8) = u32x2{vv[0][r], vv[1][r]};
        bar();
        const int nq = i16 >> 2, nb = 4 * (((nq & 1) << 1) | (nq >> 1)) + (l & 3);
        const LAS char* ep = sm + B3_EX + (2 * w + (g & 1)) * 1024 + nb * 64;
        const u32x4 a0 = *(const LAS u32x4*)(ep + ((h ^ (nb >> 2)) * 16)), a1 = *(const LAS u32x4*)(ep + (((2 + h) ^ (nb >> 2)) * 16));
        f32x16 y = {};
        y = mfma32(a0, e[0], y); y = mfma32(a1, e[1], y);
#pragma unroll
        for (int sp = 0; sp < 2; ++sp) {
            const u32x4 v = {pk(y[8 * sp], y[8 * sp + 1]), pk(y[8 * sp + 2], y[8 * sp + 3]), pk(y[8 * sp + 4], y[8 * sp + 5]), pk(y[8 * sp + 6], y[8 * sp + 7])};
            *(GAS u32x4*)(P.out + (size_t)(s_lo + 128 * (2 * w + sp)) * 2048 + 1024 + (l & 31) * 32 + h * 16) = v;
        }
    }
    WAITV(0);
    bar();
    if (w < 4) {
        const int q = i16 >> 2, p = l & 3, nt = w >> 1;
        const LAS char* wp = sm + A3_WO + (4 * h + q) * 128 + (((nt * 4 + 2 * (p & 1) + (g & 1)) ^ ((q >> 1) << 2)) * 16) + (p >> 1) * 8;
        f32x16 acc = {};
#pragma unroll
        for (int H = 0; H < 32; ++H) {
            const u32x4 a = cat(lds_tr(wp + H * 2048), lds_tr(wp + H * 2048 + 1024));
            acc = mfma32(a, of[H], acc);
        }
#pragma unroll
        for (int s = 0; s < 2; ++s) {
            const u32x4 v = {pk(acc[8 * s], acc[8 * s + 1]), pk(acc[8 * s + 2], acc[8 * s + 3]), pk(acc[8 * s + 4], acc[8 * s + 5]), pk(acc[8 * s + 6], acc[8 * s + 7])};
            *(GAS u32x4*)(P.out + (size_t)(rt32 * 32 + (l & 31)) * 2048 + ct * 128 + nt * 64 + h * 32 + s * 16) = v;
        }
    }
}

extern "C" void kernel_launch(void* const* d_in, const int* in_sizes, int n_in, void* d_out, int out_size,
                              void* d_ws, size_t ws_size, hipStream_t stream) {
    Params P;
    P.ax = (const char*)d_in[0]; P.wqkv = (const char*)d_in[1]; P.wo = (const char*)d_in[2];
    P.bx = (const char*)d_in[3]; P.wr = (const char*)d_in[4]; P.wi = (const char*)d_in[5];
    P.out = (char*)d_out; P.ws = (char*)d_ws;
    k1<<<256, 512, 0, stream>>>(P);
    k2<<<256, 512, 0, stream>>>(P);
    k3<<<256, 512, 0, stream>>>(P);
}
```

```cpp
#include <hip/hip_runtime.h>
#include <stdint.h>

#define DI __device__ __forceinline__
#define LAS __attribute__((address_space(3)))
#define GAS __attribute__((address_space(1)))
typedef __bf16 bf16x8 __attribute__((ext_vector_type(8)));
typedef __bf16 bf16x2 __attribute__((ext_vector_type(2)));
typedef short s16x4 __attribute__((ext_vector_type(4)));
typedef float f32x2 __attribute__((ext_vector_type(2)));
typedef float f32x4 __attribute__((ext_vector_type(4)));
typedef float f32x16 __attribute__((ext_vector_type(16)));
typedef unsigned u32x2 __attribute__((ext_vector_type(2)));
typedef unsigned u32x4 __attribute__((ext_vector_type(4)));
typedef unsigned short u16;
typedef unsigned long long u64;
typedef __amdgpu_buffer_rsrc_t rsrc_t;

constexpr unsigned MB = 1u << 20;
constexpr unsigned T_OFF = 0;
constexpr unsigned U_OFF = 2 * MB;
constexpr unsigned O_OFF = 4 * MB;
constexpr unsigned KV_OFF = 6 * MB;
constexpr unsigned KVREC = 2304;
constexpr unsigned AB_OFF = 7 * MB;
constexpr unsigned FL_OFF = 7 * MB + 65536;
constexpr unsigned FU_OFF = FL_OFF + 16384;
constexpr unsigned EP_OFF = 7 * MB + 131072;
constexpr unsigned WS_USED = 8 * MB;
constexpr int SPIN_CAP = 1 << 20;

constexpr int STG = 24576;
constexpr int L_WQ = 0, L_WO = 0;
constexpr int L_XIN = 98304, L_EX = L_XIN + 16 * 1152;
constexpr int L_TIN = 135168, L_WST = L_TIN + 8192, L_SPT = L_WST + 8192, L_SPA = L_SPT + 512;
constexpr int L_UIN = L_TIN, L_EX3 = 65536;
constexpr int L_M2 = L_XIN, L_M3 = L_M2 + 7168;
constexpr int L_RED = L_SPA + 2048, L_KVF = L_RED + 2 * 2304, L_END = L_KVF + 2304;
static_assert(L_EX + 16384 <= L_TIN && L_END <= 163840, "lds map");

DI unsigned pk(float lo, float hi) { f32x2 v = {lo, hi}; return __builtin_bit_cast(unsigned, __builtin_convertvector(v, bf16x2)); }
DI f32x4 mfma16(u32x4 a, u32x4 b, f32x4 c) { return __builtin_amdgcn_mfma_f32_16x16x32_bf16(__builtin_bit_cast(bf16x8, a), __builtin_bit_cast(bf16x8, b), c, 0, 0, 0); }
DI f32x16 mfma32(u32x4 a, u32x4 b, f32x16 c) { return __builtin_amdgcn_mfma_f32_32x32x16_bf16(__builtin_bit_cast(bf16x8, a), __builtin_bit_cast(bf16x8, b), c, 0, 0, 0); }
DI float cosr(float rev) { return __builtin_amdgcn_cosf(rev); }
DI float sinr(float rev) { return __builtin_amdgcn_sinf(rev); }
DI u32x2 lds_tr(const LAS char* p) { return __builtin_bit_cast(u32x2, __builtin_amdgcn_ds_read_tr16_b64_v4i16((LAS s16x4*)p)); }
DI u32x4 cat(u32x2 a, u32x2 b) { return u32x4{a.x, a.y, b.x, b.y}; }
template <int O> DI u32x4 pk8(const f32x16& a) { return u32x4{pk(a[O], a[O + 1]), pk(a[O + 2], a[O + 3]), pk(a[O + 4], a[O + 5]), pk(a[O + 6], a[O + 7])}; }
#define WAITV(n) asm volatile("s_waitcnt vmcnt(" #n ")" ::: "memory")
#define SCHED() __builtin_amdgcn_sched_barrier(0)
DI void bar() { asm volatile("s_waitcnt lgkmcnt(0)\n\ts_barrier" ::: "memory"); }
template <bool SC = false> DI void glds(const void* sbase, unsigned voff, unsigned lds) {
    unsigned keep;
    if (SC) asm volatile("s_mov_b32 %0, m0\n\ts_mov_b32 m0, %3\n\ts_nop 0\n\tglobal_load_lds_dwordx4 %1, %2 sc1\n\ts_mov_b32 m0, %0"
                         : "=&s"(keep) : "v"(voff), "s"(sbase), "s"(lds) : "memory");
    else asm volatile("s_mov_b32 %0, m0\n\ts_mov_b32 m0, %3\n\ts_nop 0\n\tglobal_load_lds_dwordx4 %1, %2\n\ts_mov_b32 m0, %0"
                      : "=&s"(keep) : "v"(voff), "s"(sbase), "s"(lds) : "memory");
}
DI void glds4sc(const void* sbase, unsigned voff, unsigned lds) {
    unsigned keep;
    asm volatile("s_mov_b32 %0, m0\n\ts_mov_b32 m0, %3\n\ts_nop 0\n\tglobal_load_lds_dword %1, %2 sc1\n\ts_mov_b32 m0, %0"
                 : "=&s"(keep) : "v"(voff), "s"(sbase), "s"(lds) : "memory");
}
DI u32x4 ld_dev(rsrc_t r, unsigned off) { return __builtin_amdgcn_raw_buffer_load_b128(r, off, 0, 16); }
DI void st_dev(rsrc_t r, unsigned off, u32x4 v) { __builtin_amdgcn_raw_buffer_store_b128(v, r, off, 0, 16); }
DI u32x4 unmark(u32x4 v) { return __builtin_elementwise_min(v, u32x4{0xfffffffeu, 0xfffffffeu, 0xfffffffeu, 0xfffffffeu}); }
DI unsigned mx4(unsigned m, u32x4 v) { return max(max(max(m, v.x), v.y), max(v.z, v.w)); }
DI bool marked(unsigned m) { return __any(m == 0xffffffffu); }
DI unsigned phi2(float a, float b) {
    const f32x2 u = f32x2{a, b} * 1.44269504f;
    const f32x2 e = {__builtin_amdgcn_fmed3f(__builtin_amdgcn_exp2f(u[0]), 0.f, 1.f), __builtin_amdgcn_fmed3f(__builtin_amdgcn_exp2f(u[1]), 0.f, 1.f)};
    const f32x2 r = f32x2{fmaxf(a, 1e-6f), fmaxf(b, 1e-6f)} + e;
    return pk(r[0], r[1]);
}
template <int O> DI u32x4 phi8(const f32x16& a) { return u32x4{phi2(a[O], a[O + 1]), phi2(a[O + 2], a[O + 3]), phi2(a[O + 4], a[O + 5]), phi2(a[O + 6], a[O + 7])}; }
DI float bf2f(unsigned v) { return __builtin_bit_cast(float, v << 16); }

struct Params {
    const char* ax; const char* wqkv; const char* wo; const char* bx; const char* wr; const char* wi;
    char* out; char* ws;
};
#define STAMP_DECL
#define STAMP(i)
#define STAMP_END
#define RETRY(i)
#define KSTAMP(i)

struct B1Mat { u32x4 e[2], gr[2], gi[2], hr, hi; };
DI void b1_mats(B1Mat& M, int s_lo, int w, int l) {
    const int h = l >> 5, g = l >> 4, i16 = l & 15;
    {
        const int c = (l & 31) >> 1, im = l & 1, cc = (im && c == 0) ? 16 : c;
        const float st = (float)cc * (1.f / 32.f), a0 = (float)(8 * h) * st + ((im && c) ? 0.25f : 0.f);
        const unsigned sg = (cc & 1) ? 0x80008000u : 0u;
#pragma unroll
        for (int j = 0; j < 4; ++j) {
            M.e[0][j] = pk(cosr(a0 + (float)(2 * j) * st), cosr(a0 + (float)(2 * j + 1) * st));
            M.e[1][j] = M.e[0][j] ^ sg;
        }
    }
    const unsigned sg = i16 < 8 ? 0x80008000u : 0u;
#pragma unroll
    for (int cc = 0; cc < 2; ++cc) {
        const int c = 2 * w + cc;
        const float st = (float)(c + 32 * i16 + ((c == 0 && i16 >= 8) ? 16 : 0)) * (1.f / 512.f);
        const float a0 = (float)(8 * (g & 1)) * st - ((g >= 2 && c) ? 0.25f : 0.f);
#pragma unroll
        for (int j = 0; j < 4; ++j) {
            const float a = a0 + (float)(2 * j) * st, b = a0 + (float)(2 * j + 1) * st;
            M.gr[cc][j] = pk(cosr(a), cosr(b));
            M.gi[cc][j] = pk(sinr(a), sinr(b)) ^ sg;
        }
    }
    if (w == 0) {
        const unsigned on = ((g >= 2) == (i16 >= 8)) ? 0xffffffffu : 0u;
#pragma unroll
        for (int j = 0; j < 4; ++j) { M.gr[0][j] &= on; M.gi[0][j] = (i16 == 0 ? 0xbf803f80u : M.gi[0][j]) & on; }
    }
    const float st = (float)i16 * (1.f / 16.f), a0 = (float)(s_lo * i16) * (1.f / 2048.f) + (float)(4 * g) * st;
#pragma unroll
    for (int j = 0; j < 4; ++j) {
        const float a = a0 + (float)j * st, co = cosr(a), si = sinr(a);
        M.hr[j] = pk(co, si); M.hi[j] = pk(-si, co);
    }
}
DI void b1_run(LAS char* sm, const B1Mat& M, int w, int l) {
    const int h = l >> 5, g = l >> 4, i16 = l & 15;
    {
        const int q = i16 >> 2, p = i16 & 3, ps = ((p & 1) << 1) | (p >> 1);
        const LAS char* xr = sm + L_XIN + (2 * w + (g & 1)) * 1152 + (8 * h + q) * 32 + ps * 8;
        const u32x4 a0 = cat(lds_tr(xr), lds_tr(xr + 128));
        const u32x4 a1 = cat(lds_tr(xr + 512), lds_tr(xr + 640));
        f32x16 acc = {};
        acc = mfma32(a0, M.e[0], acc);
        acc = mfma32(a1, M.e[1], acc);
        const int c = (l & 31) >> 1, qq = 2 * (l & 1) + h, ph = (c & 1) | ((c & 2) << 1);
        const int s = 2 * w;
        LAS char* ep = sm + L_EX + c * 1024 + (s >> 2) * 256;
        *(LAS u32x4*)(ep + ((((s & 3) * 4 + (qq ^ ((s >> 3) << 1))) ^ ph) * 16)) = pk8<0>(acc);
        *(LAS u32x4*)(ep + (((((s & 3) + 1) * 4 + (qq ^ ((s >> 3) << 1))) ^ ph) * 16)) = pk8<8>(acc);
    }
    bar();
    unsigned tv[2][4];
#pragma unroll
    for (int cc = 0; cc < 2; ++cc) {
        const int c = 2 * w + cc, ph = (c & 1) | ((c & 2) << 1), s = i16;
        const int slot = ((s & 3) * 4 + (g ^ ((s >> 3) << 1))) ^ ph;
        const u32x4 a = *(const LAS u32x4*)(sm + L_EX + c * 1024 + (s >> 2) * 256 + slot * 16);
        const f32x4 z = {};
        const f32x4 re = mfma16(a, M.gr[cc], z), im = mfma16(a, M.gi[cc], z);
        const u32x4 b = {pk(re[0], im[0]), pk(re[1], im[1]), pk(re[2], im[2]), pk(re[3], im[3])};
        const f32x4 tr = mfma16(M.hr, b, z), ti = mfma16(M.hi, b, z);
#pragma unroll
        for (int r = 0; r < 4; ++r) tv[cc][r] = pk(tr[r], ti[r]);
    }
    const int q = w >> 1;
#pragma unroll
    for (int r = 0; r < 4; ++r)
        *(LAS u32x2*)(sm + L_XIN + ((4 * g + r) * 4 + q) * 256 + ((i16 ^ (4 * q)) * 16) + (w & 1) * 8) = u32x2{tv[0][r], tv[1][r]};
    bar();
}

struct B2Mat { u32x4 f8r, f8i, f16r, f16i; f32x4 t1c, t1s, t2; };
DI void b2_matsA(B2Mat& M, int l) {
    const int g = l >> 4, i16 = l & 15;
    const float ka = (float)(i16 >> 1), fi = (float)i16;
#pragma unroll
    for (int j = 0; j < 2; ++j) {
        const float a = (float)(2 * g + j) * ka * 0.125f, co = cosr(a), si = sinr(a);
        const unsigned r = pk(co, si), i = pk(-si, co);
        M.f8r[2 * j] = (i16 & 1) ? 0u : r; M.f8r[2 * j + 1] = (i16 & 1) ? r : 0u;
        M.f8i[2 * j] = (i16 & 1) ? 0u : i; M.f8i[2 * j + 1] = (i16 & 1) ? i : 0u;
        const float a2 = (float)(2 * g + j) * fi * (1.f / 128.f);
        M.t2[j] = cosr(a2); M.t2[2 + j] = sinr(a2);
    }
    asm volatile("" : "+v"(M.f8r), "+v"(M.f8i), "+v"(M.t2));
}
DI void b2_matsB(B2Mat& M, int l) {
    const int g = l >> 4, i16 = l & 15;
    const float ka = (float)(i16 >> 1), fi = (float)i16;
#pragma unroll
    for (int j = 0; j < 4; ++j) {
        const float n = (float)(4 * g + j), a2 = n * fi * 0.0625f, c2 = cosr(a2), s2 = sinr(a2);
        M.f16r[j] = pk(c2, s2); M.f16i[j] = pk(-s2, c2);
        const float a3 = n * ka * (1.f / 128.f);
        M.t1c[j] = cosr(a3); M.t1s[j] = sinr(a3);
    }
    asm volatile("" : "+v"(M.f16r), "+v"(M.f16i), "+v"(M.t1c), "+v"(M.t1s));
}
#define PIN13(M) asm volatile("" : "+v"(M.e[0]), "+v"(M.e[1]), "+v"(M.gr[0]), "+v"(M.gr[1]), "+v"(M.gi[0]), "+v"(M.gi[1]), "+v"(M.hr), "+v"(M.hi))
DI void b2_tile(const B2Mat& M, u32x4 a, const float (&wr)[4], const float (&wi)[4], f32x4& ore, f32x4& oim) {
    const f32x4 z = {};
    f32x4 re = mfma16(a, M.f8r, z), im = mfma16(a, M.f8i, z);
    u32x4 b;
#pragma unroll
    for (int r = 0; r < 4; ++r) b[r] = pk(re[r] * M.t1c[r] + im[r] * M.t1s[r], im[r] * M.t1c[r] - re[r] * M.t1s[r]);
    re = mfma16(M.f16r, b, z); im = mfma16(M.f16i, b, z);
#pragma unroll
    for (int r = 0; r < 4; ++r) b[r] = pk(re[r] * wi[r] + im[r] * wr[r], re[r] * wr[r] - im[r] * wi[r]);
    im = mfma16(b, M.f16r, z); re = mfma16(b, M.f16i, z);
#pragma unroll
    for (int r = 0; r < 4; ++r) b[r] = pk(im[r] * M.t2[r >> 1] + re[r] * M.t2[2 + (r >> 1)], re[r] * M.t2[r >> 1] - im[r] * M.t2[2 + (r >> 1)]);
    oim = mfma16(M.f8r, b, z); ore = mfma16(M.f8i, b, z);
}

struct B3Mat { u32x4 hr, hi, gr[2], gi[2], e[2]; };
DI void b3_h(B3Mat& M, int s_lo, int l) {
    const float hs = (float)(s_lo + 128 * (l & 15)) * (1.f / 2048.f);
#pragma unroll
    for (int j = 0; j < 4; ++j) {
        const float a = (float)(4 * (l >> 4) + j) * hs, co = cosr(a), si = sinr(a);
        M.hr[j] = pk(co, -si); M.hi[j] = pk(si, co);
    }
    asm volatile("" : "+v"(M.hr), "+v"(M.hi));
}
DI void b3_e(B3Mat& M, int l) {
#pragma unroll
    for (int j = 0; j < 4; ++j)
#pragma unroll
        for (int t = 0; t < 2; ++t) {
            const float ae = (float)((l & 31) * (8 * t + 4 * (l >> 5) + j)) * (1.f / 32.f);
            M.e[t][j] = pk(cosr(ae), -sinr(ae));
        }
    if (l < 32) M.e[0][0] = (l & 1) ? 0xbf803f80u : 0x3f803f80u;
    asm volatile("" : "+v"(M.e[0]), "+v"(M.e[1]));
}
DI void b3_g(u32x4 (&gr)[2], u32x4 (&gi)[2], int w, int l) {
    const int g = l >> 4, i16 = l & 15;
    const float fi = (float)i16;
    const unsigned s1 = g < 2 ? 0x80000000u : 0u, s2 = s1 ^ 0x80000000u;
#pragma unroll
    for (int cc = 0; cc < 2; ++cc) {
        const int c = 2 * w + cc;
        const float t0 = fi * ((float)((c == 0 && g >= 2) ? 16 : c) * (1.f / 512.f) + (float)(4 * g) * 0.0625f);
#pragma unroll
        for (int j = 0; j < 4; ++j) {
            const float th = t0 + (float)j * 0.0625f * fi, c2 = cosr(th), sn = sinr(th);
            gr[cc][j] = pk(c2, sn) ^ s1; gi[cc][j] = pk(sn, c2) ^ s2;
        }
    }
    if (w == 0) {
        if (g == 0) gr[0][0] = (i16 & 1) ? 0xbf803f80u : 0x3f803f80u;
#pragma unroll
        for (int j = 0; j < 4; ++j) { gi[0][j] = g < 2 ? 0u : gr[0][j]; gr[0][j] = g < 2 ? gr[0][j] : 0u; }
    }
    asm volatile("" : "+v"(gr[0]), "+v"(gr[1]), "+v"(gi[0]), "+v"(gi[1]));
}

__global__ void __launch_bounds__(512, 2) fused(Params P) {
    __shared__ __attribute__((aligned(16))) char smem_[L_END];
    LAS char* sm = (LAS char*)smem_;
    const unsigned lds0 = (unsigned)(uintptr_t)sm;
    const int t = threadIdx.x, l = t & 63, w = __builtin_amdgcn_readfirstlane(t >> 6);
    const int b = blockIdx.x, xc = b & 7, j = b >> 3;
    const int h = l >> 5, g = l >> 4, i16 = l & 15;
    const int rb = 8 * (xc >> 1) + (j >> 2), hg = 4 * (xc & 1) + (j & 3), rot = (rb + 2 * hg) & 7;
    const int rb3 = 4 * xc + (j >> 3), ct = j & 7;
    const bool isB = j < 16;
    const int s_lo = xc * 16 + (j & 15);
    const int k_lo = xc + 8 * (j & 1), kb = j >> 1;
    const rsrc_t ws = __builtin_amdgcn_make_buffer_rsrc(P.ws, 0, WS_USED, 0x00020000);
    const u32x4 MARK = {0xffffffffu, 0xffffffffu, 0xffffffffu, 0xffffffffu};
    const bool ldr = w >= 4;
    const int lw = w & 3;
    int lwS = lw; asm volatile("" : "+s"(lwS));
    STAMP_DECL STAMP(0);

    const u64 tag = *(const __attribute__((address_space(4))) u64*)(uintptr_t)(P.ws + EP_OFF) + 1;

    const int k1ab = 16 * (8 * (j - 16) + xc) + 8 * (w & 1) + (l >> 3);
    if (!isB && (w >> 1) == 1) {
        const char* p = ((l & 4) ? P.wi : P.wr) + (size_t)((((l & 1) ? -k1ab : k1ab) & 2047) * 257 + ((l & 2) ? 256 : 0)) * 2;
        unsigned keep;
        asm volatile("s_mov_b32 %0, m0\n\ts_mov_b32 m0, %2\n\ts_nop 0\n\tglobal_load_lds_ushort %1, off\n\ts_mov_b32 m0, %0"
                     : "=&s"(keep) : "v"(p), "s"(lds0 + L_KVF + (lwS & 1) * 256) : "memory");
    }
    if (isB && ldr) {
#pragma unroll
        for (int i = 0; i < 4; ++i) glds(P.bx + (size_t)(s_lo + 128 * (4 * lwS + i)) * 1024, l * 16, lds0 + L_XIN + (4 * lwS + i) * 1152);
    }
    const unsigned xo = (rb * 64 + 16 * lw + (l >> 3)) * 1024 + (((l & 7) ^ (l >> 4)) * 16);
    const unsigned xo1 = (rb * 64 + 16 * lw + 8 + (l >> 3)) * 1024 + (((l & 7) ^ (4 + (l >> 4))) * 16);
    const unsigned wo_ = (16 * lw + (l >> 3)) * 3072 + hg * 128 + (((l & 7) ^ ((l >> 2) & 4)) * 16);
    auto stage_a = [&](int i) {
        const int s = (i + rot) & 7;
        const unsigned lb = lds0 + (i & 3) * STG + lwS * 2048;
        const char* xb = P.ax + s * 128;
        const char* wb = P.wqkv + (size_t)s * (64 * 3072) + 1024;
        glds(xb, xo, lb); glds(xb, xo1, lb + 1024);
        glds(wb, wo_, lb + 8192); glds(wb + 8 * 3072, wo_, lb + 8192 + 1024);
    };
    auto stage_b = [&](int i) {
        const unsigned lb = lds0 + (i & 3) * STG + lwS * 2048;
        const char* wb = P.wqkv + (size_t)((i + rot) & 7) * (64 * 3072) + 2048;
        glds(wb, wo_, lb + 16384); glds(wb + 8 * 3072, wo_, lb + 16384 + 1024);
    };
    auto stage = [&](int i) { stage_a(i); stage_b(i); };
    auto wqchunk = [&](int i) {
        const char* wb = P.wqkv + (size_t)((i + rot) & 7) * (64 * 3072);
        glds(wb, wo_, lds0 + L_WQ + i * 8192 + lwS * 2048); glds(wb + 8 * 3072, wo_, lds0 + L_WQ + i * 8192 + lwS * 2048 + 1024);
    };
    if (ldr) stage_a(0);
    auto clears = [&]() {
        st_dev(ws, O_OFF + ((2 * rb + (w >> 1)) * 32 + 4 * hg + 2 * (w & 1)) * 1024 + l * 16, MARK);
        st_dev(ws, O_OFF + ((2 * rb + (w >> 1)) * 32 + 4 * hg + 2 * (w & 1) + 1) * 1024 + l * 16, MARK);
        if (t < 136) st_dev(ws, KV_OFF + (hg * 32 + rb) * KVREC + t * 16, MARK);
    };
    if (isB) {
        B1Mat M;
        b1_mats(M, s_lo, w, l);
        PIN13(M);
        STAMP(1);
        if (ldr) { stage_b(0); WAITV(6); }
        bar();
        STAMP(2);
        b1_run(sm, M, w, l);
        if (!ldr) {
#pragma unroll
            for (int it = 0; it < 4; ++it) {
                const int i = t + 256 * it, ch = i >> 2, q = i & 3;
                st_dev(ws, T_OFF + ch * 8192 + s_lo * 64 + q * 16, *(const LAS u32x4*)(sm + L_XIN + ((ch >> 4) * 4 + q) * 256 + (((ch & 15) ^ (4 * q)) * 16)));
            }
            clears();
        }
        bar();
    } else if (ldr) stage_b(0);
    else if (w < 2) { __builtin_amdgcn_s_sleep(10); clears(); }
    else {
        WAITV(0);
        clears();
        float v = bf2f(*(const LAS u16*)(sm + L_KVF + (w & 1) * 256 + l * 4));
        if ((l & 5) == 5) v = -v;
        v += __builtin_bit_cast(float, __builtin_amdgcn_update_dpp(0, __builtin_bit_cast(int, v), 0xB1, 0xf, 0xf, true));
        const float o = __builtin_bit_cast(float, __builtin_amdgcn_update_dpp(0, __builtin_bit_cast(int, v), 0x4E, 0xf, 0xf, true));
        v = ((l & 2) ? o - v : v + o) * (0.25f / (2048.f * 512.f));
        if (!(l & 1)) __builtin_amdgcn_raw_buffer_store_b32(__builtin_bit_cast(unsigned, v), ws, AB_OFF + ((k1ab & 15) * 128 + (k1ab >> 4)) * 16 + (l & 2) * 4 + (l & 4), 0, 16);
    }
    STAMP(3);

    B2Mat M2;
    B3Mat M3;
    u32x4 xr[32];
    f32x16 ak = {}, av = {};
    const int q4 = i16 >> 2, p4 = l & 3;
    const int woff = (8 * h + q4) * 128 + ((((w >> 1) * 4 + 2 * (g & 1) + (p4 >> 1)) ^ ((q4 >> 1) << 2)) * 16) + (p4 & 1) * 8;
    if (ldr) {
        auto m2out = [&]() {
            if (lw == 0) {
                LAS u32x4* m = (LAS u32x4*)(sm + L_M2) + l;
                m[0] = M2.f8r; m[64] = M2.f8i; m[128] = M2.f16r; m[192] = M2.f16i;
                m[256] = __builtin_bit_cast(u32x4, M2.t1c); m[320] = __builtin_bit_cast(u32x4, M2.t1s); m[384] = __builtin_bit_cast(u32x4, M2.t2);
            }
        };
        stage(1);
        if (!isB) { b2_matsA(M2, l); b2_matsB(M2, l); m2out(); }
        WAITV(6); bar(); stage(2);
        WAITV(6); bar(); stage(3);
        if (isB) b2_matsA(M2, l);
        WAITV(6); bar(); stage(4);
        WAITV(6); bar(); stage(5);
        if (isB) { b2_matsB(M2, l); m2out(); }
        WAITV(6); bar(); stage(6);
        WAITV(6); bar(); stage(7);
        {
            const int jg = kb < 8 ? 2 * kb : 2 * (15 - kb) + 1;
            const unsigned vo = ((k_lo + 16 * (64 * (lw & 1) + (l >> 1))) * 257 + 16 * jg + 8 * (l & 1)) * 2;
            const char* wb = (lwS & 2) ? P.wi : P.wr;
            glds(wb, vo, lds0 + L_WST + lwS * 2048); glds(wb + 32 * 16 * 514, vo, lds0 + L_WST + lwS * 2048 + 1024);
        }
        WAITV(8); bar();
        {
            const char* tb = P.ws + T_OFF + (k_lo * 16 + kb) * 8192 + lwS * 2048;
            const unsigned vo = (l >> 2) * 64 + (((l & 3) ^ (l >> 4)) * 16);
            glds<true>(tb, vo, lds0 + L_TIN + lwS * 2048); glds<true>(tb + 1024, vo, lds0 + L_TIN + lwS * 2048 + 1024);
            if (kb == 0) {
                if (lwS < 2) glds4sc(P.ws + T_OFF + ((16 - k_lo) & 15) * 16 * 8192 + lwS * 4096, l * 64, lds0 + L_SPT + lwS * 256);
                else glds<true>(P.ws + AB_OFF + k_lo * 2048 + (lwS - 2) * 1024, l * 16, lds0 + L_SPA + (lwS - 2) * 1024);
            }
        }
        WAITV(4); bar();
        wqchunk(0); wqchunk(1); wqchunk(2); wqchunk(3); wqchunk(4); wqchunk(5); wqchunk(6); wqchunk(7);
        STAMP(5);
        WAITV(16); bar();
    } else {
        const int row = 32 * (w & 1) + (l & 31);
        const int xoff = row * 128, xsw = (row >> 1) & 7;
        u32x4 fl = {};
        u32x4 wk[4], wv[4];
        auto rd = [&](int st, int tt) {
            const LAS char* sb = sm + (st & 3) * STG;
            xr[4 * st + tt] = *(const LAS u32x4*)(sb + xoff + (((2 * tt + h) ^ xsw) * 16));
            const LAS char* wp = sb + 8192 + woff + tt * 2048;
            wk[tt] = cat(lds_tr(wp), lds_tr(wp + 512));
            wv[tt] = cat(lds_tr(wp + 8192), lds_tr(wp + 8192 + 512));
        };
        auto mm = [&](int st, int tt) { ak = mfma32(xr[4 * st + tt], wk[tt], ak); av = mfma32(xr[4 * st + tt], wv[tt], av); };
        bar();
        STAMP(4);
        rd(0, 0); rd(0, 1);
#pragma unroll
        for (int st = 0; st < 7; ++st) {
            rd(st, 2); rd(st, 3);
            SCHED();
            mm(st, 0); mm(st, 1);
            SCHED();
            if (st == 1) WAITV(0);
            if (st == 5 && w < 2) {
                for (int spin = 0; spin < SPIN_CAP; ++spin) {
                    const unsigned lo = (unsigned)tag, hi = (unsigned)(tag >> 32);
                    if (__all(fl.x == lo && fl.y == hi && fl.z == lo && fl.w == hi)) break;
                    __builtin_amdgcn_s_sleep(8);
                    fl = ld_dev(ws, FL_OFF + xc * 2048 + w * 1024 + l * 16);
                    RETRY(0);
                }
            }
            bar();
            KSTAMP(st);
            if (st == 1 && t < 8) __builtin_amdgcn_raw_buffer_store_b64(u32x2{(unsigned)tag, (unsigned)(tag >> 32)}, ws, FL_OFF + t * 2048 + b * 8, 0, 16);
            if (st == 3 && w < 2) fl = ld_dev(ws, FL_OFF + xc * 2048 + w * 1024 + l * 16);
            rd(st + 1, 0); rd(st + 1, 1);
            SCHED();
            mm(st, 2); mm(st, 3);
            SCHED();
        }
        rd(7, 2); rd(7, 3);
        SCHED();
#pragma unroll
        for (int tt = 0; tt < 4; ++tt) ak = mfma32(xr[28 + tt], wk[tt], ak);
#pragma unroll
        for (int tt = 0; tt < 4; ++tt) av = mfma32(xr[28 + tt], wv[tt], av);
        STAMP(5);
        const u32x4 ones = {0x3f803f80u, 0x3f803f80u, 0x3f803f80u, 0x3f803f80u};
        const u32x4 p0 = phi8<0>(ak), p1 = phi8<8>(ak);
        f32x16 kv = {}, zz = {};
        kv = mfma32(p0, pk8<0>(av), kv); zz = mfma32(p0, ones, zz);
        kv = mfma32(p1, pk8<8>(av), kv); zz = mfma32(p1, ones, zz);
        const bool hi = (l & 16) != 0;
        f32x4 k0, k1, z0, z1;
#pragma unroll
        for (int r = 0; r < 4; ++r) {
            float a0 = kv[r], a1 = kv[8 + r], b0 = kv[4 + r], b1 = kv[12 + r], c0 = zz[r], c1 = zz[8 + r], d0 = zz[4 + r], d1 = zz[12 + r];
            asm("" : "+v"(a0), "+v"(a1), "+v"(b0), "+v"(b1)); asm("" : "+v"(c0), "+v"(c1), "+v"(d0), "+v"(d1));
            k0[r] = hi ? a1 : a0; k1[r] = hi ? b1 : b0; z0[r] = hi ? c1 : c0; z1[r] = hi ? d1 : d0;
        }
        LAS f32x4* red = (LAS f32x4*)(sm + L_RED + (w >> 1) * 2304);
        const int zi = 128 + (g & 1) * 4 + h * 2;
        if (w & 1) {
            red[l] = k0; red[64 + l] = k1;
            if (i16 == 0) { red[zi] = z0; red[zi + 1] = z1; }
        }
        bar();
        {
            const LAS u32x4* m = (const LAS u32x4*)(sm + L_M2) + l;
            M2.f8r = m[0]; M2.f8i = m[64]; M2.f16r = m[128]; M2.f16i = m[192];
            M2.t1c = __builtin_bit_cast(f32x4, m[256]); M2.t1s = __builtin_bit_cast(f32x4, m[320]); M2.t2 = __builtin_bit_cast(f32x4, m[384]);
        }
        if (!(w & 1)) {
            k0 += red[l]; k1 += red[64 + l];
            const unsigned rec = KV_OFF + (hg * 32 + rb) * KVREC;
            st_dev(ws, rec + (w >> 1) * 1024 + l * 16, unmark(u32x4{pk(k0[0], k0[1]), pk(k0[2], k0[3]), pk(k1[0], k1[1]), pk(k1[2], k1[3])}));
            if (i16 == 0) {
                z0 += red[zi]; z1 += red[zi + 1];
                st_dev(ws, rec + 2048 + ((w >> 1) * 4 + (g & 1) * 2 + h) * 16, unmark(u32x4{pk(z0[0], z0[1]), pk(z0[2], z0[3]), pk(z1[0], z1[1]), pk(z1[2], z1[3])}));
            }
        }
    }
    STAMP(6);
    {
        const int c = 2 * w + (i16 & 1), col = kb < 8 ? c : ((16 - c) & 15);
        float wr[4], wi[4];
        const float sc = 2.f / (2048.f * 512.f);
#pragma unroll
        for (int r = 0; r < 4; ++r) {
            const int khi = (i16 >> 1) + 8 * (4 * g + r);
            wr[r] = bf2f(*(const LAS u16*)(sm + L_WST + khi * 32 + col * 2)) * sc;
            wi[r] = bf2f(*(const LAS u16*)(sm + L_WST + 4096 + khi * 32 + col * 2)) * sc;
        }
        LAS char* tp = sm + L_TIN + (32 * g + i16) * 64 + (((w >> 1) ^ (i16 >> 2)) * 16) + (w & 1) * 8;
        const u32x4 a = cat(*(const LAS u32x2*)tp, *(const LAS u32x2*)(tp + 1024));
        f32x4 ore, oim;
        b2_tile(M2, a, wr, wi, ore, oim);
        u32x4 o = {pk(ore[0], oim[0]), pk(ore[1], oim[1]), pk(ore[2], oim[2]), pk(ore[3], oim[3])};
        if (kb == 0 && w == 0) {
            const u32x4 sa = {a[0], *(const LAS unsigned*)(sm + L_SPT + (32 * g + i16) * 4) ^ 0x80000000u,
                              a[2], *(const LAS unsigned*)(sm + L_SPT + (32 * g + 16 + i16) * 4) ^ 0x80000000u};
            const int u = i16 & 1;
#pragma unroll
            for (int r = 0; r < 4; ++r) {
                const int khi = ((i16 >> 1) + 8 * (4 * g + r) - ((u && k_lo) ? 1 : 0)) & 127;
                const f32x2 ab = *(const LAS f32x2*)(sm + L_SPA + khi * 16 + u * 8);
                wr[r] = ab[0]; wi[r] = ab[1];
            }
            b2_tile(M2, sa, wr, wi, ore, oim);
#pragma unroll
            for (int i = 0; i < 2; ++i) {
                const float an = k_lo ? (float)(16 * (2 * g + i) + i16) * (1.f / 128.f) : 0.f;
                const float co = cosr(an), si = sinr(an);
                o[2 * i] = pk(ore[2 * i] + ore[2 * i + 1] * co + oim[2 * i + 1] * si, oim[2 * i] + oim[2 * i + 1] * co - ore[2 * i + 1] * si);
            }
        }
        *(LAS u32x2*)tp = u32x2{o[0], o[1]};
        *(LAS u32x2*)(tp + 1024) = u32x2{o[2], o[3]};
    }
    bar();
    STAMP(7);
    u32x4 pq[2];
    auto udma = [&]() {
        if (isB) {
#pragma unroll
            for (int i = 0; i < 4; ++i) glds<true>(P.ws + U_OFF + s_lo * 16384 + (4 * lwS + i) * 1024, (l >> 2) * 64 + (((l & 3) ^ (l >> 4)) * 16), lds0 + L_UIN + (4 * lwS + i) * 1024);
        }
    };
    if (!ldr) {
        if (w == 3) {
#pragma unroll
            for (int it = 0; it < 8; ++it) {
                const int i = l + 64 * it, s = i >> 2;
                st_dev(ws, U_OFF + s * 16384 + (k_lo * 16 + kb) * 64 + (((i & 3) ^ ((s >> 2) & 3)) * 16), *(const LAS u32x4*)(sm + L_TIN + i * 16));
            }
        }
        bar();
        STAMP(8);
        f32x16 aq = {};
        u32x4 wq[2][4];
        auto rdq = [&](int c) {
#pragma unroll
            for (int tt = 0; tt < 4; ++tt) { const LAS char* wp = sm + L_WQ + woff + (4 * c + tt) * 2048; wq[c & 1][tt] = cat(lds_tr(wp), lds_tr(wp + 512)); }
        };
        rdq(0);
#pragma unroll
        for (int c = 0; c < 8; ++c) {
            if (c < 7) rdq(c + 1);
            if (c == 5 && w == 3) {
                WAITV(0);
                if (l < 8) __builtin_amdgcn_raw_buffer_store_b64(u32x2{(unsigned)tag, (unsigned)(tag >> 32)}, ws, FU_OFF + l * 2048 + b * 8, 0, 16);
            }
            SCHED();
#pragma unroll
            for (int tt = 0; tt < 4; ++tt) aq = mfma32(wq[c & 1][tt], xr[4 * c + tt], aq);
            SCHED();
        }
        pq[0] = phi8<0>(aq); pq[1] = phi8<8>(aq);
    } else {
        WAITV(0);
        bar();
        STAMP(8);
        u32x4 sel;
#pragma unroll
        for (int jj = 0; jj < 4; ++jj) sel[jj] = i16 == 2 * jj ? 0x00003f80u : i16 == 2 * jj + 1 ? 0x3f800000u : 0u;
        const unsigned rec = KV_OFF + hg * 32 * KVREC;
        f32x4 s0, s1, sz;
        u32x4 v0[8], v1[8], vz[4];
        auto ldrec = [&]() {
#pragma unroll
            for (int i = 0; i < 8; ++i) {
                v0[i] = ld_dev(ws, rec + (4 * i + g) * KVREC + (32 * lw + i16) * 16);
                v1[i] = ld_dev(ws, rec + (4 * i + g) * KVREC + (32 * lw + 16 + i16) * 16);
            }
#pragma unroll
            for (int i = 0; i < 4; ++i) vz[i] = ld_dev(ws, lw == 3 ? rec + (8 * i + 2 * g + (i16 >> 3)) * KVREC + 2048 + (i16 & 7) * 16 : 0xfffffff0u);
        };
        ldrec();
        SCHED();
        if (isB) {
            b3_g(M3.gr, M3.gi, lw, l);
            LAS u32x4* m = (LAS u32x4*)(sm + L_M3 + 4096 + lw * 4096) + l;
            m[0] = M3.gr[0]; m[64] = M3.gr[1]; m[128] = M3.gi[0]; m[192] = M3.gi[1];
            b3_g(M3.gr, M3.gi, w, l);
            b3_e(M3, l);
            b3_h(M3, s_lo, l);
            if (lw == 0) { m = (LAS u32x4*)(sm + L_M3) + l; m[0] = M3.hr; m[64] = M3.hi; m[128] = M3.e[0]; m[192] = M3.e[1]; }
        }
        SCHED();
        for (int spin = 0; spin < SPIN_CAP; ++spin) {
            unsigned m = 0;
            s0 = f32x4{}; s1 = f32x4{}; sz = f32x4{};
#pragma unroll
            for (int i = 0; i < 8; ++i) { m = mx4(mx4(m, v0[i]), v1[i]); s0 = mfma16(sel, v0[i], s0); s1 = mfma16(sel, v1[i], s1); }
#pragma unroll
            for (int i = 0; i < 4; ++i) { m = mx4(m, vz[i]); sz = mfma16(sel, vz[i], sz); }
            if (!marked(m)) break;
            RETRY(1);
            ldrec();
        }
        if (l < 32) {
            *(LAS u32x2*)(sm + L_KVF + (32 * lw + i16) * 16 + g * 8) = u32x2{pk(s0[0], s0[1]), pk(s0[2], s0[3])};
            *(LAS u32x2*)(sm + L_KVF + (32 * lw + 16 + i16) * 16 + g * 8) = u32x2{pk(s1[0], s1[1]), pk(s1[2], s1[3])};
        }
        if (lw == 3) {
#pragma unroll
            for (int r = 0; r < 4; ++r) { const float x = sz[r]; sz[r] = x + __builtin_bit_cast(float, __builtin_amdgcn_update_dpp(0, __builtin_bit_cast(int, x), 0x128, 0xf, 0xf, true)); }
            if (l < 32 && i16 < 8) *(LAS u32x2*)(sm + L_KVF + 2048 + i16 * 16 + g * 8) = u32x2{pk(sz[0], sz[1]), pk(sz[2], sz[3])};
        }
    }
    STAMP(9);
    bar();
    if (!ldr) {
        const u32x4 f = *(const LAS u32x4*)(sm + L_KVF + (w >> 1) * 1024 + l * 16);
        const bool hi = (l & 16) != 0;
        const u32x4 zf = *(const LAS u32x4*)(sm + L_KVF + 2048 + ((w >> 1) * 4 + (g & 1) * 2 + h) * 16);
        if (isB) {
            const LAS u32x4* m = (const LAS u32x4*)(sm + L_M3) + l;
            M3.hr = m[0]; M3.hi = m[64]; M3.e[0] = m[128]; M3.e[1] = m[192];
            m += 256 + w * 256;
            M3.gr[0] = m[0]; M3.gr[1] = m[64]; M3.gi[0] = m[128]; M3.gi[1] = m[192];
        }
        const u32x4 zero = {};
        f32x16 num = {}, den = {};
        num = mfma32(hi ? zero : f, pq[0], num); den = mfma32(hi ? zero : zf, pq[0], den);
        num = mfma32(hi ? f : zero, pq[1], num); den = mfma32(hi ? zf : zero, pq[1], den);
        const float r0 = __builtin_amdgcn_rcpf(den[0] + 1e-6f), r1 = __builtin_amdgcn_rcpf(den[8] + 1e-6f);
#pragma unroll
        for (int i = 0; i < 8; ++i) { num[i] *= r0; num[8 + i] *= r1; }
        const unsigned oo = O_OFF + ((2 * rb + (w & 1)) * 32 + 4 * hg + 2 * (w >> 1)) * 1024 + l * 16;
        st_dev(ws, oo, unmark(pk8<0>(num)));
        st_dev(ws, oo + 1024, unmark(pk8<8>(num)));
    } else {
        if (isB) {
            for (int spin = 0; spin < SPIN_CAP; ++spin) {
                const u32x4 f0 = ld_dev(ws, FU_OFF + xc * 2048 + l * 16), f1 = ld_dev(ws, FU_OFF + xc * 2048 + 1024 + l * 16);
                const unsigned lo = (unsigned)tag, hi = (unsigned)(tag >> 32);
                if (__all(f0.x == lo && f0.y == hi && f0.z == lo && f0.w == hi && f1.x == lo && f1.y == hi && f1.z == lo && f1.w == hi)) break;
                RETRY(2);
                __builtin_amdgcn_s_sleep(8);
            }
            udma();
        }
        const unsigned vo = (l >> 3) * 1024 + (((l & 7) ^ (((l >> 4) & 3) << 1)) * 16);
#pragma unroll
        for (int m = 0; m < 16; ++m) glds(P.wo + (size_t)(16 * lwS + m) * 8192 + ct * 128, vo, lds0 + L_WO + (16 * lwS + m) * 1024);
    }
    STAMP(10);
    const unsigned ob = O_OFF + ((2 * rb3 + (w >> 1)) * 32 + (g >> 1)) * 1024 + ((g & 1) * 32 + 16 * (w & 1) + i16) * 16;
    auto a3 = [&](u32x4 (&of)[16]) __attribute__((always_inline)) {
        STAMP(12);
        const int key = (q4 >> 1) | ((g & 1) << 1);
        const LAS char* wp = sm + L_WO + (16 * (g >> 1) + 4 * (g & 1) + q4) * 128;
        const LAS char* wpn[4];
#pragma unroll
        for (int n = 0; n < 4; ++n) wpn[n] = wp + (n ^ key) * 32 + p4 * 8;
        f32x4 acc[4];
        for (int spin = 0; spin < SPIN_CAP; ++spin) {
            unsigned m = 0;
#pragma unroll
            for (int n = 0; n < 4; ++n) acc[n] = f32x4{};
#pragma unroll
            for (int s = 0; s < 16; ++s) {
                m = mx4(m, of[s]);
#pragma unroll
                for (int n = 0; n < 4; ++n) acc[n] = mfma16(cat(lds_tr(wpn[n] + s * 4096), lds_tr(wpn[n] + s * 4096 + 1024)), of[s], acc[n]);
            }
            if (!marked(m)) break;
            RETRY(3);
#pragma unroll
            for (int s = 0; s < 16; ++s) of[s] = ld_dev(ws, ob + s * 2048);
        }
        GAS char* op = (GAS char*)P.out + (size_t)(rb3 * 64 + 16 * w + i16) * 2048 + ct * 128 + (g & 1) * 32 + (g >> 1) * 16;
#pragma unroll
        for (int n = 0; n < 4; n += 2) {
            const auto lo = __builtin_amdgcn_permlane16_swap(pk(acc[n][0], acc[n][1]), pk(acc[n + 1][0], acc[n + 1][1]), false, false);
            const auto hi = __builtin_amdgcn_permlane16_swap(pk(acc[n][2], acc[n][3]), pk(acc[n + 1][2], acc[n + 1][3]), false, false);
            *(GAS u32x4*)(op + n * 32) = u32x4{lo[0], hi[0], lo[1], hi[1]};
        }
    };
    if (isB) {
        u32x4 a0, a1;
        if (ldr) WAITV(16);
        {
            bar();
            STAMP(11);
            u32x2 d[4];
#pragma unroll
            for (int i = 0; i < 4; ++i) d[i] = *(const LAS u32x2*)(sm + L_UIN + (4 * g + i) * 1024 + i16 * 64 + (((w >> 1) ^ (i16 >> 2)) * 16) + (w & 1) * 8);
            unsigned vv[2][4];
#pragma unroll
            for (int cc = 0; cc < 2; ++cc) {
                const u32x4 a = {d[0][cc], d[1][cc], d[2][cc], d[3][cc]};
                const f32x4 z = {};
                const f32x4 re = mfma16(a, M3.hr, z), im = mfma16(a, M3.hi, z);
                const u32x4 a2 = {pk(re[0], im[0]), pk(re[1], im[1]), pk(re[2], im[2]), pk(re[3], im[3])};
                const f32x4 vr = mfma16(a2, M3.gr[cc], z), vi = mfma16(a2, M3.gi[cc], z);
#pragma unroll
                for (int r = 0; r < 4; ++r) vv[cc][r] = pk(vr[r], vi[r]);
            }
#pragma unroll
            for (int r = 0; r < 4; ++r)
                *(LAS u32x2*)(sm + L_EX3 + (4 * g + r) * 1024 + i16 * 64 + (((w >> 1) ^ (i16 >> 2)) * 16) + (w & 1) * 8) = u32x2{vv[0][r], vv[1][r]};
            if (ldr) WAITV(0);
            bar();
            const int nb = 4 * (((q4 & 1) << 1) | (q4 >> 1)) + (l & 3);
            const LAS char* ep = sm + L_EX3 + (2 * w + (g & 1)) * 1024 + nb * 64;
            a0 = *(const LAS u32x4*)(ep + ((h ^ (nb >> 2)) * 16)); a1 = *(const LAS u32x4*)(ep + (((2 + h) ^ (nb >> 2)) * 16));
        }
        auto fin = [&]() __attribute__((always_inline)) {
            f32x16 y = {};
            y = mfma32(a0, M3.e[0], y); y = mfma32(a1, M3.e[1], y);
            GAS char* op = (GAS char*)P.out + (size_t)(s_lo + 256 * w) * 2048 + 1024 + (l & 31) * 32 + h * 16;
            *(GAS u32x4*)op = pk8<0>(y);
            *(GAS u32x4*)(op + 128 * 2048) = pk8<8>(y);
        };
        if (ldr) {
            if (b == 0 && t == 256) *(GAS u64*)(P.ws + EP_OFF) = tag;
            fin();
        } else {
            u32x4 of[16];
#pragma unroll
            for (int s = 0; s < 16; ++s) of[s] = ld_dev(ws, ob + s * 2048);
            SCHED();
            fin();
            a3(of);
        }
    } else if (ldr) {
        WAITV(0);
        bar();
    } else {
        WAITV(0);
        u32x4 of[16];
#pragma unroll
        for (int s = 0; s < 16; ++s) of[s] = ld_dev(ws, ob + s * 2048);
        bar();
        a3(of);
    }
    STAMP(13); STAMP_END;
}

extern "C" void kernel_launch(void* const* d_in, const int* in_sizes, int n_in, void* d_out, int out_size,
                              void* d_ws, size_t ws_size, hipStream_t stream) {
    Params P;
    P.ax = (const char*)d_in[0]; P.wqkv = (const char*)d_in[1]; P.wo = (const char*)d_in[2];
    P.bx = (const char*)d_in[3]; P.wr = (const char*)d_in[4]; P.wi = (const char*)d_in[5];
    P.out = (char*)d_out; P.ws = (char*)d_ws;
    fused<<<256, 512, 0, stream>>>(P);
}
```

```cpp
#include <hip/hip_runtime.h>
#include <stdint.h>

#define DI __device__ __forceinline__
#define LAS __attribute__((address_space(3)))
#define GAS __attribute__((address_space(1)))
typedef __bf16 bf16x8 __attribute__((ext_vector_type(8)));
typedef __bf16 bf16x2 __attribute__((ext_vector_type(2)));
typedef short s16x4 __attribute__((ext_vector_type(4)));
typedef float f32x2 __attribute__((ext_vector_type(2)));
typedef float f32x4 __attribute__((ext_vector_type(4)));
typedef float f32x16 __attribute__((ext_vector_type(16)));
typedef unsigned u32x2 __attribute__((ext_vector_type(2)));
typedef unsigned u32x4 __attribute__((ext_vector_type(4)));
typedef unsigned short u16;
typedef unsigned long long u64;
typedef __amdgpu_buffer_rsrc_t rsrc_t;

constexpr unsigned MB = 1u << 20;
constexpr unsigned T_OFF = 0;
constexpr unsigned U_OFF = T_OFF;
constexpr unsigned O_OFF = 4 * MB;
constexpr unsigned KV_OFF = 6 * MB;
constexpr unsigned KVREC = 2304;
constexpr unsigned AB_OFF = 7 * MB;
constexpr unsigned FL_OFF = 7 * MB + 262144;
constexpr unsigned EP_OFF = 7 * MB + 131072;
constexpr unsigned WS_USED = 8 * MB;
constexpr int SPIN_CAP = 1 << 20;

constexpr int L_A3X = 0;
constexpr int KSTG = 32768, L_WST = 0, L_TIN = 16384, L_KWO = 32768, L_UIN = 98304, L_EX3 = 114688;
constexpr int L_RED = 131072, L_SPT = L_RED + 2 * 4608, L_SPA = L_SPT + 512, L_SYN = L_SPA + 2048;
constexpr int QSTG = 24576, L_XIN = 98304, L_EX = L_XIN + 16 * 1152, L_ABR = L_EX + 16384, L_DUMP = L_ABR + 512, L_QSYN = L_DUMP + 256;
constexpr int L_QWO = 98304, L_KVF = 16384;
constexpr int L_END = 163840;
static_assert(L_SYN + 64 <= L_END - 256 && L_QSYN + 32 <= L_END - 256, "lds map");

DI unsigned pk(float lo, float hi) { f32x2 v = {lo, hi}; return __builtin_bit_cast(unsigned, __builtin_convertvector(v, bf16x2)); }
DI f32x4 mfma16(u32x4 a, u32x4 b, f32x4 c) { return __builtin_amdgcn_mfma_f32_16x16x32_bf16(__builtin_bit_cast(bf16x8, a), __builtin_bit_cast(bf16x8, b), c, 0, 0, 0); }
DI f32x16 mfma32(u32x4 a, u32x4 b, f32x16 c) { return __builtin_amdgcn_mfma_f32_32x32x16_bf16(__builtin_bit_cast(bf16x8, a), __builtin_bit_cast(bf16x8, b), c, 0, 0, 0); }
DI float cosr(float rev) { return __builtin_amdgcn_cosf(rev); }
DI float sinr(float rev) { return __builtin_amdgcn_sinf(rev); }
template <int N> DI void cseq(float (&c)[N], float a0, float st) {
    const float k = 2.f * cosr(st);
    c[0] = cosr(a0); c[1] = cosr(a0 + st);
#pragma unroll
    for (int n = 2; n < N; ++n) c[n] = k * c[n - 1] - c[n - 2];
}
template <int N> DI void csseq(float (&c)[N], float (&s)[N], float a0, float st) {
    const float k = 2.f * cosr(st);
    c[0] = cosr(a0); c[1] = cosr(a0 + st); s[0] = sinr(a0); s[1] = sinr(a0 + st);
#pragma unroll
    for (int n = 2; n < N; ++n) { c[n] = k * c[n - 1] - c[n - 2]; s[n] = k * s[n - 1] - s[n - 2]; }
}
DI u32x2 lds_tr(const LAS char* p) { return __builtin_bit_cast(u32x2, __builtin_amdgcn_ds_read_tr16_b64_v4i16((LAS s16x4*)p)); }
DI u32x4 cat(u32x2 a, u32x2 b) { return u32x4{a.x, a.y, b.x, b.y}; }
template <int O> DI u32x4 pk8(const f32x16& a) { return u32x4{pk(a[O], a[O + 1]), pk(a[O + 2], a[O + 3]), pk(a[O + 4], a[O + 5]), pk(a[O + 6], a[O + 7])}; }
#define WAITV(n) asm volatile("s_waitcnt vmcnt(" #n ")" ::: "memory")
#define SCHED() __builtin_amdgcn_sched_barrier(0)
DI void bar() { asm volatile("s_waitcnt lgkmcnt(0)\n\ts_barrier" ::: "memory"); }
template <bool SC = false> DI void glds(const void* sbase, unsigned voff, unsigned lds) {
    unsigned keep;
    if (SC) asm volatile("s_mov_b32 %0, m0\n\ts_mov_b32 m0, %3\n\ts_nop 0\n\tglobal_load_lds_dwordx4 %1, %2 sc1\n\ts_mov_b32 m0, %0"
                         : "=&s"(keep) : "v"(voff), "s"(sbase), "s"(lds) : "memory");
    else asm volatile("s_mov_b32 %0, m0\n\ts_mov_b32 m0, %3\n\ts_nop 0\n\tglobal_load_lds_dwordx4 %1, %2\n\ts_mov_b32 m0, %0"
                      : "=&s"(keep) : "v"(voff), "s"(sbase), "s"(lds) : "memory");
}
DI void glds4sc(const void* sbase, unsigned voff, unsigned lds) {
    unsigned keep;
    asm volatile("s_mov_b32 %0, m0\n\ts_mov_b32 m0, %3\n\ts_nop 0\n\tglobal_load_lds_dword %1, %2 sc1\n\ts_mov_b32 m0, %0"
                 : "=&s"(keep) : "v"(voff), "s"(sbase), "s"(lds) : "memory");
}
DI u32x4 ld_dev(rsrc_t r, unsigned off) { return __builtin_amdgcn_raw_buffer_load_b128(r, off, 0, 16); }
DI void st_dev(rsrc_t r, unsigned off, u32x4 v) { __builtin_amdgcn_raw_buffer_store_b128(v, r, off, 0, 16); }
DI u32x4 unmark(u32x4 v) { return __builtin_elementwise_min(v, u32x4{0xfffffffeu, 0xfffffffeu, 0xfffffffeu, 0xfffffffeu}); }
DI unsigned mx4(unsigned m, u32x4 v) { return max(max(max(m, v.x), v.y), max(v.z, v.w)); }
DI bool marked(unsigned m) { return __any(m == 0xffffffffu); }
DI unsigned phi2(float a, float b) {
    const f32x2 u = f32x2{a, b} * 1.44269504f;
    const f32x2 e = {__builtin_amdgcn_fmed3f(__builtin_amdgcn_exp2f(u[0]), 0.f, 1.f), __builtin_amdgcn_fmed3f(__builtin_amdgcn_exp2f(u[1]), 0.f, 1.f)};
    const f32x2 r = f32x2{__builtin_amdgcn_fmed3f(a, 1e-6f, 3.0e38f), __builtin_amdgcn_fmed3f(b, 1e-6f, 3.0e38f)} + e;
    return pk(r[0], r[1]);
}
template <int O> DI u32x4 phi8(const f32x16& a) { return u32x4{phi2(a[O], a[O + 1]), phi2(a[O + 2], a[O + 3]), phi2(a[O + 4], a[O + 5]), phi2(a[O + 6], a[O + 7])}; }
DI float bf2f(unsigned v) { return __builtin_bit_cast(float, v << 16); }

struct Params {
    const char* ax; const char* wqkv; const char* wo; const char* bx; const char* wr; const char* wi;
    char* out; char* ws;
};
#define STAMP_DECL
#define STAMP(i)
#define STAMP_END
#define RETRY(i)
#define QSTAMP(i) STAMP(i)
#define PRE_S0 4
#define OCLR 3
#define KLSTAMP(i) STAMP(i)
#define PFQ 0
#define LOOK 1
#define OROT 1
#define WSPLIT 3
#define OMK 0
#define KSTAMP(i)

struct B1Mat { u32x4 e[2], gr[2], gi[2], hr, hi; };
DI void b1_mats(B1Mat& M, int s_lo, int w, int l) {
    const int h = l >> 5, g = l >> 4, i16 = l & 15;
    {
        const int c = (l & 31) >> 1, im = l & 1, cc = (im && c == 0) ? 16 : c;
        const float st = (float)cc * (1.f / 32.f), a0 = (float)(8 * h) * st + ((im && c) ? 0.25f : 0.f);
        const unsigned sg = (cc & 1) ? 0x80008000u : 0u;
        float cv[8];
        cseq(cv, a0, st);
#pragma unroll
        for (int j = 0; j < 4; ++j) { M.e[0][j] = pk(cv[2 * j], cv[2 * j + 1]); M.e[1][j] = M.e[0][j] ^ sg; }
    }
    const unsigned sg = i16 < 8 ? 0x80008000u : 0u;
#pragma unroll
    for (int cc = 0; cc < 2; ++cc) {
        const int c = 2 * w + cc;
        const float st = (float)(c + 32 * i16 + ((c == 0 && i16 >= 8) ? 16 : 0)) * (1.f / 512.f);
        const float a0 = (float)(8 * (g & 1)) * st - ((g >= 2 && c) ? 0.25f : 0.f);
        float cv[8], sv[8];
        csseq(cv, sv, a0, st);
#pragma unroll
        for (int j = 0; j < 4; ++j) { M.gr[cc][j] = pk(cv[2 * j], cv[2 * j + 1]); M.gi[cc][j] = pk(sv[2 * j], sv[2 * j + 1]) ^ sg; }
    }
    if (w == 0) {
        const unsigned on = ((g >= 2) == (i16 >= 8)) ? 0xffffffffu : 0u;
#pragma unroll
        for (int j = 0; j < 4; ++j) { M.gr[0][j] &= on; M.gi[0][j] = (i16 == 0 ? 0xbf803f80u : M.gi[0][j]) & on; }
    }
    const float st = (float)i16 * (1.f / 16.f), a0 = (float)(s_lo * i16) * (1.f / 2048.f) + (float)(4 * g) * st;
    float cv[4], sv[4];
    csseq(cv, sv, a0, st);
#pragma unroll
    for (int j = 0; j < 4; ++j) { M.hr[j] = pk(cv[j], sv[j]); M.hi[j] = pk(-sv[j], cv[j]); }
}
DI void b1_a(LAS char* sm, const B1Mat& M, int w, int l) {
    const int h = l >> 5, g = l >> 4, i16 = l & 15;
    {
        const int q = i16 >> 2, p = i16 & 3, ps = ((p & 1) << 1) | (p >> 1);
        const LAS char* xr = sm + L_XIN + (2 * w + (g & 1)) * 1152 + (8 * h + q) * 32 + ps * 8;
        const u32x4 a0 = cat(lds_tr(xr), lds_tr(xr + 128));
        const u32x4 a1 = cat(lds_tr(xr + 512), lds_tr(xr + 640));
        f32x16 acc = {};
        acc = mfma32(a0, M.e[0], acc);
        acc = mfma32(a1, M.e[1], acc);
        const int c = (l & 31) >> 1, qq = 2 * (l & 1) + h, ph = (c & 1) | ((c & 2) << 1);
        const int s = 2 * w;
        LAS char* ep = sm + L_EX + c * 1024 + (s >> 2) * 256;
        *(LAS u32x4*)(ep + ((((s & 3) * 4 + (qq ^ ((s >> 3) << 1))) ^ ph) * 16)) = pk8<0>(acc);
        *(LAS u32x4*)(ep + (((((s & 3) + 1) * 4 + (qq ^ ((s >> 3) << 1))) ^ ph) * 16)) = pk8<8>(acc);
    }
}
DI void b1_b(LAS char* sm, const B1Mat& M, int w, int l) {
    const int g = l >> 4, i16 = l & 15;
    unsigned tv[2][4];
#pragma unroll
    for (int cc = 0; cc < 2; ++cc) {
        const int c = 2 * w + cc, ph = (c & 1) | ((c & 2) << 1), s = i16;
        const int slot = ((s & 3) * 4 + (g ^ ((s >> 3) << 1))) ^ ph;
        const u32x4 a = *(const LAS u32x4*)(sm + L_EX + c * 1024 + (s >> 2) * 256 + slot * 16);
        const f32x4 z = {};
        const f32x4 re = mfma16(a, M.gr[cc], z), im = mfma16(a, M.gi[cc], z);
        const u32x4 b = {pk(re[0], im[0]), pk(re[1], im[1]), pk(re[2], im[2]), pk(re[3], im[3])};
        const f32x4 tr = mfma16(M.hr, b, z), ti = mfma16(M.hi, b, z);
#pragma unroll
        for (int r = 0; r < 4; ++r) tv[cc][r] = pk(tr[r], ti[r]);
    }
    const int q = w >> 1;
#pragma unroll
    for (int r = 0; r < 4; ++r)
        *(LAS u32x2*)(sm + L_XIN + ((4 * g + r) * 4 + q) * 256 + ((i16 ^ (4 * q)) * 16) + (w & 1) * 8) = u32x2{tv[0][r] & ~1u, tv[1][r]};
}

struct B2Mat { u32x4 f8r, f8i, f16r, f16i; f32x4 t1c, t1s, t2c, t2s; };
constexpr float BSCALE = 2.f / (2048.f * 512.f);
DI void b2_matsA(B2Mat& M, int l) {
    const int g = l >> 4, i16 = l & 15;
    const float ka = (float)(i16 >> 1), fi = (float)i16;
#pragma unroll
    for (int j = 0; j < 2; ++j) {
        const float a = (float)(2 * g + j) * ka * 0.125f, co = cosr(a), si = sinr(a);
        const unsigned r = pk(co, si), i = pk(-si, co);
        M.f8r[2 * j] = (i16 & 1) ? 0u : r; M.f8r[2 * j + 1] = (i16 & 1) ? r : 0u;
        M.f8i[2 * j] = (i16 & 1) ? 0u : i; M.f8i[2 * j + 1] = (i16 & 1) ? i : 0u;
        const float a2 = (float)(2 * g + j) * fi * (1.f / 128.f);
        M.t2c[2 * j] = M.t2c[2 * j + 1] = cosr(a2) * BSCALE; M.t2s[2 * j] = M.t2s[2 * j + 1] = sinr(a2) * BSCALE;
    }
    asm volatile("" : "+v"(M.f8r), "+v"(M.f8i), "+v"(M.t2c), "+v"(M.t2s));
}
DI void b2_matsB(B2Mat& M, int l) {
    const int g = l >> 4, i16 = l & 15;
    const float s16 = (float)i16 * 0.0625f, s128 = (float)(i16 >> 1) * (1.f / 128.f), n0 = (float)(4 * g);
    float c[4], d[4], e[4], f[4];
    csseq(c, d, n0 * s16, s16);
    csseq(e, f, n0 * s128, s128);
#pragma unroll
    for (int j = 0; j < 4; ++j) { M.f16r[j] = pk(c[j], d[j]); M.f16i[j] = pk(-d[j], c[j]); M.t1c[j] = e[j]; M.t1s[j] = f[j]; }
    asm volatile("" : "+v"(M.f16r), "+v"(M.f16i), "+v"(M.t1c), "+v"(M.t1s));
}
#define PIN13(M) asm volatile("" : "+v"(M.e[0]), "+v"(M.e[1]), "+v"(M.gr[0]), "+v"(M.gr[1]), "+v"(M.gi[0]), "+v"(M.gi[1]), "+v"(M.hr), "+v"(M.hi))
DI f32x2 lo2(f32x4 v) { return __builtin_shufflevector(v, v, 0, 1); }
DI f32x2 hi2(f32x4 v) { return __builtin_shufflevector(v, v, 2, 3); }
DI u32x4 rot4(f32x4 x, f32x4 y, f32x4 c, f32x4 s) {
    const f32x2 a0 = __builtin_elementwise_fma(lo2(x), lo2(c), lo2(y) * lo2(s)), a1 = __builtin_elementwise_fma(hi2(x), hi2(c), hi2(y) * hi2(s));
    const f32x2 b0 = __builtin_elementwise_fma(lo2(y), lo2(c), -(lo2(x) * lo2(s))), b1 = __builtin_elementwise_fma(hi2(y), hi2(c), -(hi2(x) * hi2(s)));
    return u32x4{pk(a0[0], b0[0]), pk(a0[1], b0[1]), pk(a1[0], b1[0]), pk(a1[1], b1[1])};
}
struct B2Tile {
    f32x4 re, im, wr, wi; u32x4 b;
    DI void pin() { asm volatile("" : "+v"(re), "+v"(im)); }
    DI void m1(const B2Mat& M) { re = mfma16(b, M.f8r, f32x4{}); im = mfma16(b, M.f8i, f32x4{}); pin(); }
    DI void v1(const B2Mat& M) { b = rot4(re, im, M.t1c, M.t1s); }
    DI void m2(const B2Mat& M) { re = mfma16(M.f16r, b, f32x4{}); im = mfma16(M.f16i, b, f32x4{}); pin(); }
    DI void v2() { b = rot4(im, re, wr, wi); }
    DI void m3(const B2Mat& M) { im = mfma16(b, M.f16r, f32x4{}); re = mfma16(b, M.f16i, f32x4{}); pin(); }
    DI void v3(const B2Mat& M) { b = rot4(im, re, M.t2c, M.t2s); }
    DI void m4(const B2Mat& M) { im = mfma16(M.f8r, b, f32x4{}); re = mfma16(M.f8i, b, f32x4{}); pin(); }
};

struct B3Mat { u32x4 hr, hi, gr[2], gi[2], e[2]; };
DI void b3_h(B3Mat& M, int s_lo, int l) {
    const float hs = (float)(s_lo + 128 * (l & 15)) * (1.f / 2048.f);
    float c[4], d[4];
    csseq(c, d, (float)(4 * (l >> 4)) * hs, hs);
#pragma unroll
    for (int j = 0; j < 4; ++j) { M.hr[j] = pk(c[j], -d[j]); M.hi[j] = pk(d[j], c[j]); }
    asm volatile("" : "+v"(M.hr), "+v"(M.hi));
}
DI void b3_e(B3Mat& M, int l) {
    const float st = (float)(l & 31) * (1.f / 32.f);
#pragma unroll
    for (int t = 0; t < 2; ++t) {
        float c[4], d[4];
        csseq(c, d, (float)(8 * t + 4 * (l >> 5)) * st, st);
#pragma unroll
        for (int j = 0; j < 4; ++j) M.e[t][j] = pk(c[j], -d[j]);
    }
    if (l < 32) M.e[0][0] = (l & 1) ? 0xbf803f80u : 0x3f803f80u;
    asm volatile("" : "+v"(M.e[0]), "+v"(M.e[1]));
}
DI void b3_g(u32x4 (&gr)[2], u32x4 (&gi)[2], int w, int l) {
    const int g = l >> 4, i16 = l & 15;
    const float fi = (float)i16;
    const unsigned s1 = g < 2 ? 0x80000000u : 0u, s2 = s1 ^ 0x80000000u;
#pragma unroll
    for (int cc = 0; cc < 2; ++cc) {
        const int c = 2 * w + cc;
        float cv[4], sv[4];
        csseq(cv, sv, fi * ((float)((c == 0 && g >= 2) ? 16 : c) * (1.f / 512.f) + (float)(4 * g) * 0.0625f), 0.0625f * fi);
#pragma unroll
        for (int j = 0; j < 4; ++j) { gr[cc][j] = pk(cv[j], sv[j]) ^ s1; gi[cc][j] = pk(sv[j], cv[j]) ^ s2; }
    }
    if (w == 0) {
        if (g == 0) gr[0][0] = (i16 & 1) ? 0xbf803f80u : 0x3f803f80u;
#pragma unroll
        for (int j = 0; j < 4; ++j) { gi[0][j] = g < 2 ? 0u : gr[0][j]; gr[0][j] = g < 2 ? gr[0][j] : 0u; }
    }
    asm volatile("" : "+v"(gr[0]), "+v"(gr[1]), "+v"(gi[0]), "+v"(gi[1]));
}

#define CODE_LINES 232
constexpr int CODE_SHARE = (CODE_LINES + 31) / 32, L_CPF = L_END - 256;
DI void pfl(const void* p, unsigned lds) {
    unsigned keep;
    asm volatile("s_mov_b32 %0, m0\n\ts_mov_b32 m0, %2\n\ts_nop 0\n\tglobal_load_lds_dword %1, off\n\ts_mov_b32 m0, %0" : "=&s"(keep) : "v"(p), "s"(lds) : "memory");
}
DI void refetch(u32x4& v, rsrc_t r, unsigned off) { if (mx4(0, v) == 0xffffffffu) v = ld_dev(r, off); }

__global__ void __launch_bounds__(512, 2) fused(Params P) {
    __shared__ __attribute__((aligned(16))) char smem_[L_END];
    {
        u64 pc; asm volatile("s_getpc_b64 %0" : "=s"(pc));
        const int ln = (blockIdx.x >> 3) * CODE_SHARE + (threadIdx.x & 63);
        if (threadIdx.x < CODE_SHARE && ln < CODE_LINES) pfl((const char*)(pc & ~255ull) + (size_t)ln * 128, (unsigned)(uintptr_t)(LAS char*)smem_ + L_CPF);
    }
    LAS char* sm = (LAS char*)smem_;
    const unsigned lds0 = (unsigned)(uintptr_t)sm;
    const int t = threadIdx.x, l = t & 63, w = __builtin_amdgcn_readfirstlane(t >> 6);
    const int b = blockIdx.x, xc = b & 7, j = b >> 3, jj = j & 15;
    const int h = l >> 5, g = l >> 4, i16 = l & 15, q4 = i16 >> 2, p4 = l & 3;
    const bool isQ = j < 16;
    const int rb = 4 * (xc >> 1) + (jj >> 2), hg = 4 * (xc & 1) + (jj & 3);
    const int rb3 = 4 * xc + (j >> 3), ct = j & 7;
    const int s_lo = xc * 16 + jj;
    const rsrc_t ws = __builtin_amdgcn_make_buffer_rsrc(P.ws, 0, WS_USED, 0x00020000);
    const rsrc_t outr = __builtin_amdgcn_make_buffer_rsrc(P.out, 0, 4 * MB, 0x00020000);
    const u32x4 MARK = {0xffffffffu, 0xffffffffu, 0xffffffffu, 0xffffffffu};
    const bool ldr = w >= 4;
    const int lw = w & 3;
    int wS = w; asm volatile("" : "+s"(wS));
    const int lwS = wS & 3;
    STAMP_DECL STAMP(0);
    const u64 tag = *(const __attribute__((address_space(4))) u64*)(uintptr_t)(P.ws + EP_OFF) + 1;
    const u32x2 tg = {(unsigned)tag, (unsigned)(tag >> 32)};
    auto setflag = [&](int kind) __attribute__((always_inline)) { if (l < 8) __builtin_amdgcn_raw_buffer_store_b64(tg, ws, FL_OFF + l * 65536 + kind * 8192 + (hg * 16 + rb) * 64, 0, 16); };
    auto ldflag = [&](unsigned off) __attribute__((always_inline)) { return __builtin_amdgcn_raw_buffer_load_b64(ws, off, 0, 16); };
    auto floff = [&](int kind, int idx, bool on) __attribute__((always_inline)) { return on ? FL_OFF + xc * 65536 + kind * 8192 + idx * 64 : 0xfffffff0u; };
    auto waitflag = [&](u32x2 f, unsigned off) __attribute__((always_inline)) {
        bool m = off != 0xfffffff0u && (f.x != tg.x || f.y != tg.y);
#pragma unroll 1
        for (int spin = 0; spin < SPIN_CAP && __any(m); ++spin) {
            RETRY(0);
            __builtin_amdgcn_s_sleep(3);
            f = ldflag(m ? off : 0xfffffff0u);
            m = m && (f.x != tg.x || f.y != tg.y);
        }
    };
    const unsigned xoE = (rb * 128 + (l >> 3)) * 1024 + (((l & 7) ^ (l >> 4)) * 16);
    const unsigned xoO = (rb * 128 + 8 + (l >> 3)) * 1024 + (((l & 7) ^ (4 + (l >> 4))) * 16);
    const unsigned wo_ = (l >> 3) * 3072 + hg * 128 + (((l & 7) ^ ((l >> 2) & 4)) * 16);
    const int wh = (j >> 3) & 1, wobase = isQ ? L_QWO : L_KWO;
    auto wodma = [&](int second, int w0, int n) __attribute__((always_inline)) {
        const unsigned vo = (l >> 3) * 1024 + (((l & 7) ^ (((l >> 4) & 3) << 1)) * 16);
#pragma unroll
        for (int m = 0; m < n; ++m) glds(P.wo + (size_t)(32 * (wh ^ second) + n * w0 + m) * 8192 + ct * 128, vo, lds0 + wobase + (32 * second + n * w0 + m) * 1024);
    };
    const int kh = w >> 2;
    const unsigned ob = O_OFF + ((2 * rb3 + (lw >> 1)) * 32 + 16 * kh + (g >> 1)) * 1024 + ((g & 1) * 32 + 16 * (lw & 1) + i16) * 16;
    auto a3pre = [&](u32x4 (&wf)[8][4]) __attribute__((always_inline)) {
        const int key = (q4 >> 1) | ((g & 1) << 1);
        const LAS char* wp = sm + wobase + (kh ^ wh) * 32768 + (16 * (g >> 1) + 4 * (g & 1) + q4) * 128 + p4 * 8;
#pragma unroll
        for (int n = 0; n < 4; ++n)
#pragma unroll
            for (int s = 0; s < 8; ++s) wf[s][n] = cat(lds_tr(wp + (n ^ key) * 32 + s * 4096), lds_tr(wp + (n ^ key) * 32 + s * 4096 + 1024));
    };
    auto a3ld = [&](u32x4 (&of)[8]) __attribute__((always_inline)) {
#pragma unroll
        for (int s = 0; s < 8; ++s) of[s] = ld_dev(ws, ob + s * 2048);
    };
    auto a3 = [&](u32x4 (&of)[8], u32x4 (&wf)[8][4]) __attribute__((always_inline)) {
        STAMP(12);
        f32x4 acc[4];
        auto pass = [&]() __attribute__((always_inline)) {
            unsigned m = 0;
#pragma unroll
            for (int n = 0; n < 4; ++n) acc[n] = f32x4{};
#pragma unroll
            for (int s = 0; s < 8; ++s) {
                m = mx4(m, of[s]);
#pragma unroll
                for (int n = 0; n < 4; ++n) acc[n] = mfma16(wf[s][n], of[s], acc[n]);
                asm volatile("" : "+v"(acc[0]), "+v"(acc[1]), "+v"(acc[2]), "+v"(acc[3]), "+v"(m));
            }
            return marked(m);
        };
        if (__builtin_expect(pass(), 0)) {
#pragma unroll 1
            for (int spin = 0; spin < SPIN_CAP; ++spin) {
                RETRY(3);
                unsigned m = 0;
#pragma unroll
                for (int s = 0; s < 8; ++s) refetch(of[s], ws, ob + s * 2048);
#pragma unroll
                for (int s = 0; s < 8; ++s) m = mx4(m, of[s]);
                if (!marked(m)) break;
                __builtin_amdgcn_s_sleep(1);
            }
            pass();
        }
        LAS f32x4* xo = (LAS f32x4*)(sm + L_A3X) + w * 128 + l;
        const LAS f32x4* xi = (const LAS f32x4*)(sm + L_A3X) + (w ^ 4) * 128 + l;
        if (ldr) { xo[0] = acc[0]; xo[64] = acc[1]; } else { xo[0] = acc[2]; xo[64] = acc[3]; }
        bar();
        f32x4 u0 = xi[0], u1 = xi[64];
        if (ldr) { u0 += acc[2]; u1 += acc[3]; } else { u0 += acc[0]; u1 += acc[1]; }
        const auto lo = __builtin_amdgcn_permlane16_swap(pk(u0[0], u0[1]), pk(u1[0], u1[1]), false, false);
        const auto hi = __builtin_amdgcn_permlane16_swap(pk(u0[2], u0[3]), pk(u1[2], u1[3]), false, false);
        st_dev(outr, (rb3 * 64 + 16 * lw + i16) * 2048 + ct * 128 + (w >> 2) * 64 + (g & 1) * 32 + (g >> 1) * 16, u32x4{lo[0], hi[0], lo[1], hi[1]});
    };

    if (isQ) {
        {
            const char* base = P.bx + (size_t)(s_lo + 256 * wS) * 1024;
            glds(base, l * 16, lds0 + L_XIN + (2 * wS) * 1152);
            glds(base + 128 * 1024, l * 16, lds0 + L_XIN + (2 * wS + 1) * 1152);
        }
        const char* pfp; unsigned pfs;
        {
            const char* x2 = P.ax; const char* w2 = P.wqkv; asm volatile("" : "+v"(x2), "+v"(w2));
            pfp = l < 32 ? x2 + (size_t)(rb * 128 + 32 * (jj & 3) + l) * 1024 : w2 + (size_t)(16 * (jj >> 2) + i16) * 3072 + ((l & 16) ? 2048 : 1024) + hg * 128;
            pfs = l < 32 ? 128 : 64 * 3072;
        }
        auto pf = [&](int s) __attribute__((always_inline)) { pfl(pfp + (size_t)(s * pfs), lds0 + L_DUMP); };
        if (w < 2) pf(1 + w);
        if (t == 0) *(LAS unsigned*)(sm + L_QSYN) = 0;
        B1Mat M;
        b1_mats(M, s_lo, w, l);
        PIN13(M);
        STAMP(1);
        if (w < 2) WAITV(1); else WAITV(0);
        STAMP(2);
        const int k1ab = 16 * s_lo + 8 * (w & 1) + (l >> 3);
        if (w < 4) pf(3 + w);
        b1_a(sm, M, w, l);
        bar();
        QSTAMP(12);
        if (w == 4) pf(7);
        if (w < 2) {
            const char* p = ((l & 4) ? P.wi : P.wr) + (size_t)((((l & 1) ? -k1ab : k1ab) & 2047) * 257 + ((l & 2) ? 256 : 0)) * 2;
            unsigned keep;
            asm volatile("s_mov_b32 %0, m0\n\ts_mov_b32 m0, %2\n\ts_nop 0\n\tglobal_load_lds_ushort %1, off\n\ts_mov_b32 m0, %0"
                         : "=&s"(keep) : "v"(p), "s"(lds0 + L_ABR + (wS & 1) * 256) : "memory");
        }
        b1_b(sm, M, w, l);
        bar();
        QSTAMP(7);
        const int qrot = PFQ ? 0 : 2 * (jj >> 2);
        auto stage = [&](int i, int part) __attribute__((always_inline)) {
            const unsigned lb = lds0 + (i & 3) * QSTG;
            const int ks = (i + qrot) & 7;
            const char* wb = P.wqkv + (size_t)(ks * 64 + 16 * lwS) * 3072;
            if (part & 1) { glds(wb, wo_, lb + 16384 + lwS * 2048); glds(wb + 8 * 3072, wo_, lb + 16384 + lwS * 2048 + 1024); }
            const char* xb = P.ax + ks * 128 + lwS * 32768;
            if (part & 2) { glds(xb, xoE, lb + lwS * 4096); glds(xb, xoO, lb + lwS * 4096 + 1024);
            glds(xb + 16384, xoE, lb + lwS * 4096 + 2048); glds(xb + 16384, xoO, lb + lwS * 4096 + 3072); }
        };
        if (!ldr) {
#pragma unroll
            for (int it = 0; it < 4; ++it) {
                const int i = t + 256 * it, q = i & 3, mm = (i >> 3) & 7, kb = (i & 4) ? 15 - mm : mm;
                st_dev(ws, T_OFF + (i >> 3) * 16384 + ((s_lo + 3 * (i >> 3)) & 127) * 128 + (i & 7) * 16, *(const LAS u32x4*)(sm + L_XIN + ((i >> 6) * 4 + q) * 256 + ((kb ^ (4 * q)) * 16)));
            }
            if (w < 2) WAITV(4);
        if (w < 2)
            {
                float v = bf2f(*(const LAS u16*)(sm + L_ABR + (w & 1) * 256 + l * 4));
                if ((l & 5) == 5) v = -v;
                v += __builtin_bit_cast(float, __builtin_amdgcn_update_dpp(0, __builtin_bit_cast(int, v), 0xB1, 0xf, 0xf, true));
                const float o = __builtin_bit_cast(float, __builtin_amdgcn_update_dpp(0, __builtin_bit_cast(int, v), 0x4E, 0xf, 0xf, true));
                v = ((l & 2) ? o - v : v + o) * 0.125f;
                if (!(l & 1)) __builtin_amdgcn_raw_buffer_store_b32(__builtin_bit_cast(unsigned, v), ws, AB_OFF + k1ab * 16 + (l & 2) * 4 + (l & 4), 0, 16);
            }
            asm volatile("s_barrier" ::: "memory");
            QSTAMP(8);
            WAITV(0);
            QSTAMP(11);
            if (__builtin_amdgcn_readfirstlane(l == 0 ? __hip_atomic_fetch_add((LAS unsigned*)(sm + L_QSYN), 1u, __ATOMIC_RELAXED, __HIP_MEMORY_SCOPE_WORKGROUP) : 0u) == 3) setflag(0);
            if (w == 2) { const unsigned fo1 = floff(1, hg * 16 + l, l < 16); waitflag(ldflag(fo1), fo1); }
            asm volatile("s_barrier" ::: "memory");
        } else {
            asm volatile("s_barrier" ::: "memory");
            stage(0, 1); stage(1, 1);
            asm volatile("s_barrier" ::: "memory");
            stage(0, 2); stage(1, 2);
        }
        auto stage8 = [&](int i) __attribute__((always_inline)) {
            const unsigned lb = lds0 + (i & 3) * QSTG;
            const int ks = (i + qrot) & 7;
            const char* xb = P.ax + ks * 128 + wS * 16384;
            glds(xb, xoE, lb + wS * 2048); glds(xb, xoO, lb + wS * 2048 + 1024);
            glds(P.wqkv + (size_t)(ks * 64 + 8 * wS) * 3072, wo_, lb + 16384 + wS * 1024);
        };
        STAMP(3);
        const int xoff = (32 * lw + (l & 31)) * 128, xsw = (l >> 1) & 7;
        const int woff = 16384 + (8 * h + q4) * 128 + ((((w >> 2) * 4 + 2 * (g & 1) + (p4 >> 1)) ^ ((q4 >> 1) << 2)) * 16) + (p4 & 1) * 8;
        f32x16 aq = {};
        u32x4 xa[4], wq[4];

        auto rd = [&](int st, int tt) __attribute__((always_inline)) {
            const LAS char* sb = sm + (st & 3) * QSTG;
            xa[tt] = *(const LAS u32x4*)(sb + xoff + (((2 * tt + h) ^ xsw) * 16));
            wq[tt] = cat(lds_tr(sb + woff + tt * 2048), lds_tr(sb + woff + tt * 2048 + 512));
        };
        auto mm = [&](int tt) __attribute__((always_inline)) { SCHED(); aq = mfma32(wq[tt], xa[tt], aq); asm volatile("" : "+v"(aq)); SCHED(); };
        if (ldr) WAITV(4);
        bar();
        STAMP(4);
        if (ldr) stage(2, 3);
        rd(0, 0); rd(0, 1);
#pragma unroll
        for (int st = 0; st < 7; ++st) {
            mm(0); rd(st, 2);
            mm(1); rd(st, 3);
            SCHED();
            if (st == 0) { if (ldr) WAITV(6); }
            else if (st == 1) { if (ldr) { if (OMK == 0) WAITV(5); else WAITV(3); } }
            else if (st == OMK + 1 && st < 6) WAITV(5);
            else if (st < 6) WAITV(3);
            else if (OMK == 4) WAITV(2);
            else WAITV(0);
            asm volatile("s_barrier" ::: "memory");
            KSTAMP(st);
            mm(2); rd(st + 1, 0);
            mm(3); rd(st + 1, 1);
            SCHED();
            if (st < 5) stage8(st + 3);
            if (st == OMK) {
#pragma unroll
                for (int i = 0; i < 2; ++i) st_dev(ws, O_OFF + ((4 * rb + lw) * 32 + 4 * hg + 2 * (w >> 2) + i) * 1024 + l * 16, MARK);
            }
        }
        mm(0); rd(7, 2);
        mm(1); rd(7, 3);
        mm(2); mm(3);
        STAMP(5);
        const unsigned rec = KV_OFF + hg * 16 * KVREC;
        u32x4 v0[4], vz[2];
        auto ldrec = [&]() __attribute__((always_inline)) {
#pragma unroll
            for (int i = 0; i < 4; ++i) v0[i] = ld_dev(ws, rec + (4 * i + g) * KVREC + (16 * w + i16) * 16);
#pragma unroll
            for (int i = 0; i < 2; ++i) vz[i] = ld_dev(ws, w == 7 ? rec + (8 * i + 2 * g + (i16 >> 3)) * KVREC + 2048 + (i16 & 7) * 16 : 0xfffffff0u);
        };
        ldrec();
        SCHED();
        u32x4 pq0 = phi8<0>(aq), pq1 = phi8<8>(aq);
        asm volatile("" : "+v"(pq0), "+v"(pq1));
        STAMP(6);
        u32x4 sel;
#pragma unroll
        for (int jx = 0; jx < 4; ++jx) sel[jx] = i16 == 2 * jx ? 0x00003f80u : i16 == 2 * jx + 1 ? 0x3f800000u : 0u;
        f32x4 s0, sz;
#pragma unroll 1
        for (int spin = 0; spin < SPIN_CAP; ++spin) {
            unsigned m = 0;
            s0 = f32x4{}; sz = f32x4{};
#pragma unroll
            for (int i = 0; i < 4; ++i) { m = mx4(m, v0[i]); s0 = mfma16(sel, v0[i], s0); }
#pragma unroll
            for (int i = 0; i < 2; ++i) { m = mx4(m, vz[i]); sz = mfma16(sel, vz[i], sz); }
            if (!marked(m)) break;
            RETRY(1);
            __builtin_amdgcn_s_sleep(2);
#pragma unroll
            for (int i = 0; i < 4; ++i) refetch(v0[i], ws, rec + (4 * i + g) * KVREC + (16 * w + i16) * 16);
#pragma unroll
            for (int i = 0; i < 2; ++i) refetch(vz[i], ws, rec + (8 * i + 2 * g + (i16 >> 3)) * KVREC + 2048 + (i16 & 7) * 16);
        }
        __builtin_amdgcn_s_waitcnt(0x0f70);
        if (l < 32) *(LAS u32x2*)(sm + L_KVF + (16 * w + i16) * 16 + g * 8) = u32x2{pk(s0[0], s0[1]), pk(s0[2], s0[3])};
        if (w == 7) {
#pragma unroll
            for (int r = 0; r < 4; ++r) { const float x = sz[r]; sz[r] = x + __builtin_bit_cast(float, __builtin_amdgcn_update_dpp(0, __builtin_bit_cast(int, x), 0x128, 0xf, 0xf, true)); }
            if (l < 32 && i16 < 8) *(LAS u32x2*)(sm + L_KVF + 2048 + i16 * 16 + g * 8) = u32x2{pk(sz[0], sz[1]), pk(sz[2], sz[3])};
        }
        wodma(0, wS, 4);
        STAMP(9);
        bar();
        if (w == 0) setflag(3);
        wodma(1, wS, 4);
        {
            const u32x4 f = *(const LAS u32x4*)(sm + L_KVF + (w >> 2) * 1024 + l * 16);
            const bool hi = (l & 16) != 0;
            const u32x4 zf = *(const LAS u32x4*)(sm + L_KVF + 2048 + ((w >> 2) * 4 + (g & 1) * 2 + h) * 16);
            const u32x4 zero = {};
            f32x16 num = {}, den = {};
            num = mfma32(hi ? zero : f, pq0, num); den = mfma32(hi ? zero : zf, pq0, den);
            num = mfma32(hi ? f : zero, pq1, num); den = mfma32(hi ? zf : zero, pq1, den);
            const float r0 = __builtin_amdgcn_rcpf(den[0] + 1e-6f), r1 = __builtin_amdgcn_rcpf(den[8] + 1e-6f);
#pragma unroll
            for (int i = 0; i < 8; ++i) { num[i] *= r0; num[8 + i] *= r1; }
            const unsigned oo = O_OFF + ((4 * rb + lw) * 32 + 4 * hg + 2 * (w >> 2)) * 1024 + l * 16;
            st_dev(ws, oo, unmark(pk8<0>(num)));
            st_dev(ws, oo + 1024, unmark(pk8<8>(num)));
        }
        STAMP(10);
        WAITV(2);
        bar();
        u32x4 of[8], wf[8][4];
        const unsigned fo3 = floff(3, l * 16 + (rb3 >> 1), l < 8);
        const u32x2 fl3 = ldflag(fo3);
        a3pre(wf);
        waitflag(fl3, fo3);
        WAITV(0);
        a3ld(of);
        a3(of, wf);
    } else {
        const int k_lo = xc + 8 * (jj & 1), mq = jj >> 1;
        auto stage = [&](int i) __attribute__((always_inline)) {
            const unsigned lb = lds0 + (i & 3) * KSTG;
            const char* xb = P.ax + i * 128 + lwS * 32768;
            glds(xb, xoE, lb + lwS * 4096); glds(xb, xoO, lb + lwS * 4096 + 1024);
            glds(xb + 16384, xoE, lb + lwS * 4096 + 2048); glds(xb + 16384, xoO, lb + lwS * 4096 + 3072);
            const char* wb = P.wqkv + (size_t)(i * 64 + 16 * lwS) * 3072 + 1024;
            glds(wb, wo_, lb + 16384 + lwS * 2048); glds(wb + 8 * 3072, wo_, lb + 16384 + lwS * 2048 + 1024);
            glds(wb + 1024, wo_, lb + 24576 + lwS * 2048); glds(wb + 1024 + 8 * 3072, wo_, lb + 24576 + lwS * 2048 + 1024);
        };
        if (ldr) stage(0);
        if (t < 8) *(LAS unsigned*)(sm + L_SYN + t * 4) = 0;
        const unsigned rec = KV_OFF + (hg * 16 + rb) * KVREC, zrec = i16 == 0 ? rec + 2048 + ((w >> 1) * 4 + (g & 1) * 2 + h) * 16 : 0xfffffff0u;
        if (w == 0 || w == 2) { st_dev(ws, rec + (w >> 1) * 1024 + l * 16, MARK); st_dev(ws, zrec, MARK); }
        B2Mat M2;
        b2_matsA(M2, l); b2_matsB(M2, l);
        if (ldr) stage(1);
        STAMP(1);
        if (ldr) {
            WAITV(8); bar(); stage(2);
            WAITV(8); bar(); stage(3);
            WAITV(8); bar(); stage(4);
            WAITV(8); bar(); stage(5);
            WAITV(8); bar(); stage(6);
            WAITV(8); bar(); stage(7);
            WAITV(8); bar();
            const unsigned f0 = floff(0, 64 * lw + l, lw < 2);
            const unsigned wvo = ((k_lo + 16 * (64 * (lw & 1) + (l >> 2))) * 257 + 32 * mq + 8 * ((l & 3) ^ ((l >> 3) & 3))) * 2;
            const char* wb = (lwS & 2) ? P.wi : P.wr;
#pragma unroll
            for (int i = 0; i < WSPLIT; ++i) glds(wb + i * 16 * 16 * 514, wvo, lds0 + L_WST + lwS * 4096 + i * 1024);
            asm volatile("s_waitcnt vmcnt(%0)\n\ts_barrier" :: "n"(WSPLIT) : "memory");
            STAMP(4);
            u32x2 fl0;
            asm volatile("buffer_load_dwordx2 %0, %1, %2, 0 offen sc1" : "=v"(fl0) : "v"(f0), "s"(ws) : "memory");
#pragma unroll
            for (int i = WSPLIT; i < 4; ++i) glds(wb + i * 16 * 16 * 514, wvo, lds0 + L_WST + lwS * 4096 + i * 1024);
            asm volatile("s_waitcnt vmcnt(%1)" : "+v"(fl0) : "n"(4 - WSPLIT) : "memory");
            volatile LAS unsigned* syn0 = (volatile LAS unsigned*)(sm + L_SYN + 16);
            if (lw < 2) {
                waitflag(fl0, f0);
                syn0[lw] = 1;
            }
#pragma unroll 1
            for (int spin = 0; spin < SPIN_CAP * 16; ++spin) { if (__builtin_amdgcn_readfirstlane(syn0[0] & syn0[1])) break; __builtin_amdgcn_s_sleep(1); }
            KLSTAMP(10);
            {
                const char* tb = P.ws + T_OFF + (k_lo * 8 + mq) * 16384;
                const int s0 = 32 * lw + (l >> 3) + 3 * (k_lo * 8 + mq);
#pragma unroll
                for (int i = 0; i < 4; ++i)
                    glds<true>(tb, ((s0 + 8 * i) & 127) * 128 + (((l & 7) ^ (4 * (i & 1) + (l >> 4))) * 16), lds0 + L_TIN + lwS * 4096 + i * 1024);
                if (mq == 0) {
                    if (lw < 2) glds4sc(P.ws + T_OFF + ((16 - k_lo) & 15) * 8 * 16384, ((64 * lw + l + 24 * ((16 - k_lo) & 15)) & 127) * 128, lds0 + L_SPT + lwS * 256);
                    else glds<true>(P.ws + AB_OFF + k_lo * 16 + (lwS - 2) * 16384, l * 256, lds0 + L_SPA + (lwS - 2) * 1024);
                }
            }
            WAITV(0);
            if (__builtin_amdgcn_readfirstlane(l == 0 ? __hip_atomic_fetch_add((LAS unsigned*)(sm + L_SYN + 8), 1u, __ATOMIC_RELAXED, __HIP_MEMORY_SCOPE_WORKGROUP) : 0u) == 3) { setflag(2); syn0[2] = 1; }
#pragma unroll 1
            for (int spin = 0; spin < SPIN_CAP * 16; ++spin) if (__builtin_amdgcn_readfirstlane(syn0[2])) break;
            wodma(0, lwS, 8);
            STAMP(5);
        } else {
            const int xoff = (64 * (w & 1) + (l & 31)) * 128, xsw = (l >> 1) & 7;
            const int woff = 16384 + (8 * h + q4) * 128 + ((((w >> 1) * 4 + 2 * (g & 1) + (p4 >> 1)) ^ ((q4 >> 1) << 2)) * 16) + (p4 & 1) * 8;
            f32x16 ak0 = {}, ak1 = {}, av0 = {}, av1 = {};
            u32x4 x0[4], x1[4], wk[4], wv[4];
            auto rdx = [&](int st, int tt) __attribute__((always_inline)) {
                const LAS char* sb = sm + (st & 3) * KSTG;
                x0[tt] = *(const LAS u32x4*)(sb + xoff + (((2 * tt + h) ^ xsw) * 16));
                x1[tt] = *(const LAS u32x4*)(sb + xoff + 4096 + (((2 * tt + h) ^ xsw) * 16));
            };
            auto rdk = [&](int st, int tt) __attribute__((always_inline)) { const LAS char* wp = sm + (st & 3) * KSTG + woff + tt * 2048; wk[tt] = cat(lds_tr(wp), lds_tr(wp + 512)); };
            auto rdv = [&](int st, int tt) __attribute__((always_inline)) { const LAS char* wp = sm + (st & 3) * KSTG + woff + tt * 2048 + 8192; wv[tt] = cat(lds_tr(wp), lds_tr(wp + 512)); };
            auto rd = [&](int st, int tt) __attribute__((always_inline)) { rdx(st, tt); rdk(st, tt); rdv(st, tt); };
#define PINA(a) asm volatile("" : "+v"(a))
            auto mm = [&](int tt, int rs, int rt) __attribute__((always_inline)) {
                SCHED(); ak0 = mfma32(x0[tt], wk[tt], ak0); PINA(ak0); SCHED(); rdx(rs, rt);
                SCHED(); ak1 = mfma32(x1[tt], wk[tt], ak1); PINA(ak1); SCHED(); rdk(rs, rt);
                SCHED(); av0 = mfma32(x0[tt], wv[tt], av0); PINA(av0); SCHED(); rdv(rs, rt);
                SCHED(); av1 = mfma32(x1[tt], wv[tt], av1); PINA(av1); SCHED();
            };
            bar();
            STAMP(2);
            rd(0, 0); rd(0, 1);
#pragma unroll
            for (int st = 0; st < 7; ++st) {
                mm(0, st, 2);
                mm(1, st, 3);
                if (st == 0) WAITV(0);
                asm volatile("s_barrier" ::: "memory");
                KSTAMP(st);
                if (st == 0 && w == 0) setflag(1);
                mm(2, st + 1, 0);
                mm(3, st + 1, 1);
            }
            rd(7, 2); rd(7, 3);
            SCHED();
#pragma unroll
            for (int tt = 0; tt < 4; ++tt) ak0 = mfma32(x0[tt], wk[tt], ak0);
#pragma unroll
            for (int tt = 0; tt < 4; ++tt) ak1 = mfma32(x1[tt], wk[tt], ak1);
#pragma unroll
            for (int tt = 0; tt < 4; ++tt) av0 = mfma32(x0[tt], wv[tt], av0);
#pragma unroll
            for (int tt = 0; tt < 4; ++tt) av1 = mfma32(x1[tt], wv[tt], av1);
            STAMP(4);
            const u32x4 ones = {0x3f803f80u, 0x3f803f80u, 0x3f803f80u, 0x3f803f80u};
            const u32x4 p0 = phi8<0>(ak0), p1 = phi8<8>(ak0), p2 = phi8<0>(ak1), p3 = phi8<8>(ak1);
            f32x16 kv = {}, zz = {};
            zz = mfma32(p0, ones, zz); kv = mfma32(p0, pk8<0>(av0), kv);
            zz = mfma32(p1, ones, zz); kv = mfma32(p1, pk8<8>(av0), kv);
            zz = mfma32(p2, ones, zz); kv = mfma32(p2, pk8<0>(av1), kv);
            zz = mfma32(p3, ones, zz); kv = mfma32(p3, pk8<8>(av1), kv);
            const bool hi = (l & 16) != 0;
            f32x4 k0, k1, z0, z1;
#pragma unroll
            for (int r = 0; r < 4; ++r) {
                float a0 = kv[r], a1 = kv[8 + r], b0 = kv[4 + r], b1 = kv[12 + r], c0 = zz[r], c1 = zz[8 + r], d0 = zz[4 + r], d1 = zz[12 + r];
                asm("" : "+v"(a0), "+v"(a1), "+v"(b0), "+v"(b1)); asm("" : "+v"(c0), "+v"(c1), "+v"(d0), "+v"(d1));
                k0[r] = hi ? a1 : a0; k1[r] = hi ? b1 : b0; z0[r] = hi ? c1 : c0; z1[r] = hi ? d1 : d0;
            }
            LAS f32x4* red = (LAS f32x4*)(sm + L_RED + (w >> 1) * 4608);
            volatile LAS unsigned* syn = (volatile LAS unsigned*)(sm + L_SYN + (w >> 1) * 4);
            if (w & 1) {
                red[l] = k0; red[64 + l] = k1; red[128 + l] = z0; red[192 + l] = z1;
                asm volatile("s_waitcnt lgkmcnt(0)" ::: "memory");
                *syn = 1;
            } else {
                for (int spin = 0; spin < SPIN_CAP * 16; ++spin) if (__builtin_amdgcn_readfirstlane(*syn)) break;
                asm volatile("" ::: "memory");
                k0 += red[l]; k1 += red[64 + l]; z0 += red[128 + l]; z1 += red[192 + l];
                st_dev(ws, rec + (w >> 1) * 1024 + l * 16, unmark(u32x4{pk(k0[0], k0[1]), pk(k0[2], k0[3]), pk(k1[0], k1[1]), pk(k1[2], k1[3])}));
                st_dev(ws, zrec, unmark(u32x4{pk(z0[0], z0[1]), pk(z0[2], z0[3]), pk(z1[0], z1[1]), pk(z1[2], z1[3])}));
            }
            STAMP(5);
        }
        bar();
        STAMP(6);
        {
            B2Tile A, B;
            LAS char* tpa = sm + L_TIN + (32 * g + i16) * 128 + (((w >> 1) ^ (i16 >> 1)) * 16) + (w & 1) * 8;
            LAS char* tpb = sm + L_TIN + (32 * g + i16) * 128 + (((4 + (w >> 1)) ^ (i16 >> 1)) * 16) + (w & 1) * 8;
            const int c = 2 * w + (i16 & 1), cb = 16 + ((16 - c) & 15);
            const LAS char* wpa = sm + L_WST + ((i16 >> 1) + 32 * g) * 64 + (((c >> 3) ^ (i16 >> 2)) * 16) + (c & 7) * 2;
            const LAS char* wpb = sm + L_WST + ((i16 >> 1) + 32 * g) * 64 + (((cb >> 3) ^ (i16 >> 2)) * 16) + (cb & 7) * 2;
            A.b = cat(*(const LAS u32x2*)tpa, *(const LAS u32x2*)(tpa + 2048));
            B.b = cat(*(const LAS u32x2*)tpb, *(const LAS u32x2*)(tpb + 2048));
            const u32x4 a0 = A.b;
            unsigned ra[8], rb_[8];
#pragma unroll
            for (int r = 0; r < 4; ++r) { ra[r] = *(const LAS u16*)(wpa + r * 512); ra[4 + r] = *(const LAS u16*)(wpa + 8192 + r * 512); }
#pragma unroll
            for (int r = 0; r < 4; ++r) { rb_[r] = *(const LAS u16*)(wpb + r * 512); rb_[4 + r] = *(const LAS u16*)(wpb + 8192 + r * 512); }
            SCHED(); A.m1(M2); B.m1(M2);
            SCHED(); A.v1(M2); SCHED(); A.m2(M2);
            SCHED(); B.v1(M2); SCHED(); B.m2(M2);
            SCHED();
#pragma unroll
            for (int r = 0; r < 4; ++r) { A.wr[r] = bf2f(ra[r]); A.wi[r] = bf2f(ra[4 + r]); }
            A.v2(); SCHED(); A.m3(M2);
            SCHED();
#pragma unroll
            for (int r = 0; r < 4; ++r) { B.wr[r] = bf2f(rb_[r]); B.wi[r] = bf2f(rb_[4 + r]); }
            B.v2(); SCHED(); B.m3(M2);
            SCHED(); A.v3(M2); SCHED(); A.m4(M2);
            SCHED(); B.v3(M2); SCHED(); B.m4(M2);
            SCHED();
            u32x4 oa = {pk(A.re[0], A.im[0]), pk(A.re[1], A.im[1]), pk(A.re[2], A.im[2]), pk(A.re[3], A.im[3])};
            *(LAS u32x2*)tpb = u32x2{pk(B.re[0], B.im[0]) | 1u, pk(B.re[1], B.im[1])};
            *(LAS u32x2*)(tpb + 2048) = u32x2{pk(B.re[2], B.im[2]) | 1u, pk(B.re[3], B.im[3])};
            if (mq == 0 && w == 0) {
                A.b = u32x4{a0[0], *(const LAS unsigned*)(sm + L_SPT + (32 * g + i16) * 4) ^ 0x80000000u,
                            a0[2], *(const LAS unsigned*)(sm + L_SPT + (32 * g + 16 + i16) * 4) ^ 0x80000000u};
                const int u = i16 & 1;
#pragma unroll
                for (int r = 0; r < 4; ++r) {
                    const int khi = ((i16 >> 1) + 8 * (4 * g + r) - ((u && k_lo) ? 1 : 0)) & 127;
                    const f32x2 ab = *(const LAS f32x2*)(sm + L_SPA + khi * 16 + u * 8);
                    A.wr[r] = ab[0]; A.wi[r] = ab[1];
                }
                A.m1(M2); A.v1(M2); A.m2(M2); A.v2(); A.m3(M2); A.v3(M2); A.m4(M2);
#pragma unroll
                for (int i = 0; i < 2; ++i) {
                    const float an = k_lo ? (float)(16 * (2 * g + i) + i16) * (1.f / 128.f) : 0.f;
                    const float co = cosr(an), sn = sinr(an);
                    oa[2 * i] = pk(A.re[2 * i] + A.re[2 * i + 1] * co + A.im[2 * i + 1] * sn, A.im[2 * i] + A.im[2 * i + 1] * co - A.re[2 * i + 1] * sn);
                }
            }
            *(LAS u32x2*)tpa = u32x2{oa[0] | 1u, oa[1]};
            *(LAS u32x2*)(tpa + 2048) = u32x2{oa[2] | 1u, oa[3]};
        }
        if (w == 1 || w == 2) { const unsigned f2 = floff(2, 64 * (w - 1) + l, 64 * (w - 1) + l != hg * 16 + rb); waitflag(ldflag(f2), f2); }
        bar();
        STAMP(7);
#pragma unroll
        for (int it = 0; it < 2; ++it) {
            const int i = t + 512 * it, s = i >> 3;
            st_dev(ws, U_OFF + s * 16384 + ((k_lo * 8 + mq + 3 * s) & 127) * 128 + (((i & 7) ^ ((s >> 1) & 7)) * 16), *(const LAS u32x4*)(sm + L_TIN + i * 16));
        }
        if (b == 128 && t == 0) __builtin_amdgcn_raw_buffer_store_b64(tg, ws, EP_OFF, 0, 16);
        wodma(1, wS, 4);
        STAMP(8);
        B3Mat M3;
        b3_g(M3.gr, M3.gi, w, l); b3_e(M3, l); b3_h(M3, s_lo, l);
        STAMP(9);
        WAITV(0);
        u32x4 wf[8][4];
        {
            const unsigned uo = U_OFF + s_lo * 16384 + (l & 4) * 16 + (l & 3) * 16;
            const unsigned o0 = uo + ((8 * w + (l >> 3) + 3 * s_lo) & 127) * 128, o1 = uo + ((64 + 8 * w + (l >> 3) + 3 * s_lo) & 127) * 128;
            u32x4 u0 = ld_dev(ws, o0), u1 = ld_dev(ws, o1);
            const unsigned fo3 = floff(3, l * 16 + (rb3 >> 1), l < 8);
            const u32x2 fl3 = ldflag(fo3);
            bar();
            a3pre(wf);
            SCHED();
#pragma unroll 1
            for (int spin = 0; spin < SPIN_CAP; ++spin) {
                const bool m0 = !(u0.x & u0.z & 1u), m1 = !(u1.x & u1.z & 1u);
                if (!__any(m0 || m1)) break;
                RETRY(2);
                __builtin_amdgcn_s_sleep(1);
                if (m0) u0 = ld_dev(ws, o0);
                if (m1) u1 = ld_dev(ws, o1);
            }
            waitflag(fl3, fo3);
            LAS char* up = sm + L_UIN + w * 1024 + (l >> 2) * 64 + (((l & 3) ^ (l >> 4)) * 16);
            *(LAS u32x4*)up = u0; *(LAS u32x4*)(up + 8192) = u1;
        }
        u32x4 a0, a1, of[8];
        const int ch = i16 < 8 ? 2 * i16 : 31 - 2 * i16;
        {
            bar();
            STAMP(11);
            a3ld(of);
            u32x2 d[4];
#pragma unroll
            for (int i = 0; i < 4; ++i) d[i] = *(const LAS u32x2*)(sm + L_UIN + (4 * g + i) * 1024 + ch * 64 + (((w >> 1) ^ (ch >> 2)) * 16) + (w & 1) * 8);
            unsigned vv[2][4];
#pragma unroll
            for (int cc = 0; cc < 2; ++cc) {
                const u32x4 a = {d[0][cc], d[1][cc], d[2][cc], d[3][cc]};
                const f32x4 z = {};
                const f32x4 re = mfma16(a, M3.hr, z), im = mfma16(a, M3.hi, z);
                const u32x4 a2 = {pk(re[0], im[0]), pk(re[1], im[1]), pk(re[2], im[2]), pk(re[3], im[3])};
                const f32x4 vr = mfma16(a2, M3.gr[cc], z), vi = mfma16(a2, M3.gi[cc], z);
#pragma unroll
                for (int r = 0; r < 4; ++r) vv[cc][r] = pk(vr[r], vi[r]);
            }
#pragma unroll
            for (int r = 0; r < 4; ++r)
                *(LAS u32x2*)(sm + L_EX3 + (4 * g + r) * 1024 + i16 * 64 + (((w >> 1) ^ (i16 >> 2)) * 16) + (w & 1) * 8) = u32x2{vv[0][r], vv[1][r]};
            bar();
            const int nb = 4 * (((q4 & 1) << 1) | (q4 >> 1)) + (l & 3);
            const LAS char* ep = sm + L_EX3 + (2 * w + (g & 1)) * 1024 + nb * 64;
            a0 = *(const LAS u32x4*)(ep + ((h ^ (nb >> 2)) * 16)); a1 = *(const LAS u32x4*)(ep + (((2 + h) ^ (nb >> 2)) * 16));
        }
        {
            f32x16 y = {};
            y = mfma32(a0, M3.e[0], y); y = mfma32(a1, M3.e[1], y);
            const unsigned op = (s_lo + 256 * w) * 2048 + 1024 + (l & 31) * 32 + h * 16;
            st_dev(outr, op, pk8<0>(y));
            st_dev(outr, op + 128 * 2048, pk8<8>(y));
        }
        a3(of, wf);
    }
    STAMP(13); STAMP_END;
}

extern "C" void kernel_launch(void* const* d_in, const int* in_sizes, int n_in, void* d_out, int out_size,
                              void* d_ws, size_t ws_size, hipStream_t stream) {
    Params P;
    P.ax = (const char*)d_in[0]; P.wqkv = (const char*)d_in[1]; P.wo = (const char*)d_in[2];
    P.bx = (const char*)d_in[3]; P.wr = (const char*)d_in[4]; P.wi = (const char*)d_in[5];
    P.out = (char*)d_out; P.ws = (char*)d_ws;
    fused<<<256, 512, 0, stream>>>(P);
}
```
